```python
import torch

def _softmax(x, axis=-1):
    return torch.softmax(x, dim=axis)
def _gelu(x):
    return torch.nn.functional.gelu(x, approximate="tanh")
def _silu(x):
    return torch.nn.functional.silu(x)
def _sigmoid(x):
    return torch.sigmoid(x)
def _layernorm(x, axis=-1, eps=1e-5):
    m = x.mean(dim=axis, keepdim=True)
    v = x.var(dim=axis, keepdim=True, unbiased=False)
    return (x - m) / torch.sqrt(v + eps)
def _scatter_add(tgt, idx, src, axis=0):
    return tgt.clone().index_add_(axis, idx, src)
def _segment_sum(src, seg, n):
    out = torch.zeros((n,) + src.shape[1:], dtype=src.dtype)
    return out.index_add_(0, seg, src)
def _pad(x, pw):
    flat = tuple(v for pair in reversed(pw) for v in pair)
    return torch.nn.functional.pad(x, flat)

kernel_name = 'interleave_layers__sliding_block_cached__mixer_channel_mlp__decode'

def setup_inputs():

    _w = (lambda a: (lambda t: t.to(torch.bfloat16) if t.is_floating_point() else t)(torch.as_tensor(a)))
    def _A_setup():
        B, Sq, D = 4, 1, 512
        Hq, Hkv, Sk, Dh = 32, 8, 8192, 128
        assert Hq % Hkv == 0, (Hq, Hkv)
        return {
            "x": _w((lambda *s: torch.randn(*s, dtype=torch.bfloat16) * 0.05)(B, Sq, D)),
            "k_cache": _w((lambda *s: torch.randn(*s, dtype=torch.bfloat16) * 1.6)(B, Hkv, Sk, Dh)),
            "v_cache": _w((lambda *s: torch.randn(*s, dtype=torch.bfloat16) * 1.6)(B, Hkv, Sk, Dh)),
            "wq": _w((lambda *s: torch.randn(*s, dtype=torch.bfloat16) * 1.6)(D, Hq * Dh) * (D ** -0.5)),
            "wo": _w((lambda *s: torch.randn(*s, dtype=torch.bfloat16) * 1.6)(Hq * Dh, D) * ((Hq * Dh) ** -0.5)),
        }
    _A_in = _A_setup()

    def _B_setup():
        B, S, D, F = 4, 1, 512, 1792
        return {
            "x": _w((lambda *s: torch.randn(*s, dtype=torch.bfloat16) * 0.05)(B, S, D)),
            "w1": _w((lambda *s: torch.randn(*s, dtype=torch.bfloat16) * 3.2)(D, F) * (D ** -0.5)),
            "b1": _w((lambda *s: torch.randn(*s, dtype=torch.bfloat16) * 1.6)(F)),
            "w2": _w((lambda *s: torch.randn(*s, dtype=torch.bfloat16) * 0.2)(F, D) * (F ** -0.5)),
            "b2": _w((lambda *s: torch.randn(*s, dtype=torch.bfloat16) * 0.2)(D)),
        }
    _B_in = _B_setup()

    return {
        "A_x": _A_in["x"],
        "A_k_cache": _A_in["k_cache"],
        "A_v_cache": _A_in["v_cache"],
        "A_wq": _A_in["wq"],
        "A_wo": _A_in["wo"],
        "B_x": _B_in["x"],
        "B_w1": _B_in["w1"],
        "B_b1": _B_in["b1"],
        "B_w2": _B_in["w2"],
        "B_b2": _B_in["b2"],
    }

def reference(A_x, A_k_cache, A_v_cache, A_wq, A_wo, B_x, B_w1, B_b1, B_w2, B_b2):
    def _A_ref(x, k_cache, v_cache, wq, wo):
        B, Sq, D = x.shape
        Hq, Dh = 32, 128
        Hkv, Sk = k_cache.shape[1], k_cache.shape[2]
        W = min(4096, max(256, Sk // 4))
        rep = Hq // Hkv
        q = (lambda x, *d: x.permute(*d))((x @ wq).reshape(B, Sq, Hq, Dh), 0, 2, 1, 3)
        k = (lambda x, n, axis: torch.repeat_interleave(x, n, dim=axis))(k_cache[:, :, Sk - W:, :], rep, axis=1)
        v = (lambda x, n, axis: torch.repeat_interleave(x, n, dim=axis))(v_cache[:, :, Sk - W:, :], rep, axis=1)
        scores = torch.einsum("bhqd,bhkd->bhqk", q, k) * (Dh ** -0.5)
        p = _softmax(scores, axis=-1)
        o = torch.einsum("bhqk,bhkd->bhqd", p, v)
        return (lambda x, *d: x.permute(*d))(o, 0, 2, 1, 3).reshape(B, Sq, Hq * Dh) @ wo

    def _B_ref(x, w1, b1, w2, b2):
        y = _layernorm(x, axis=-1)
        h = _gelu(torch.einsum("bsd,df->bsf", y, w1) + b1)
        return torch.einsum("bsf,fd->bsd", h, w2) + b2

    x = A_x
    x = x + _A_ref(_layernorm(x, axis=-1), A_k_cache, A_v_cache, A_wq, A_wo)
    x = x + _B_ref(_layernorm(x, axis=-1), B_w1, B_b1, B_w2, B_b2)
    x = x + _A_ref(_layernorm(x, axis=-1), A_k_cache, A_v_cache, A_wq, A_wo)
    x = x + _B_ref(_layernorm(x, axis=-1), B_w1, B_b1, B_w2, B_b2)
    return x
```

```cpp
#include <hip/hip_runtime.h>
#include <stdint.h>

#define DIAG 0

typedef unsigned u32;
typedef unsigned long long u64;
typedef unsigned short u16;
typedef u32 v2u __attribute__((ext_vector_type(2)));
typedef u32 v4u __attribute__((ext_vector_type(4)));
typedef float f32x2 __attribute__((ext_vector_type(2)));
typedef float f32x4 __attribute__((ext_vector_type(4)));
typedef __bf16 bf16x2 __attribute__((ext_vector_type(2)));
typedef __bf16 bf16x8 __attribute__((ext_vector_type(8)));

constexpr int D = 512, HKV = 8, SK = 8192, DH = 128, WIN = 2048, NQ = 4096, F = 1792;
constexpr u32 SENT = 0xFFFFFFFFu;
constexpr u32 SPIN_CAP = 1u << 18;
constexpr float EPS = 1e-5f;

constexpr int EP_OFF = 0;
constexpr int Q1_OFF = 1024;
constexpr int Q2_OFF = Q1_OFF + 32 * 4096;
constexpr int AO_OFF = Q2_OFF + 32 * 1024;
constexpr int AS_OFF = AO_OFF + 2 * 32 * 8 * 2048;
constexpr int WO_OFF = AS_OFF + 2 * 32 * 256;
constexpr int H_OFF = WO_OFF + 2 * 4 * 8 * 2048;
constexpr int X2_OFF = H_OFF + 2 * 4 * 4096;
constexpr int WS_BYTES = X2_OFF + 4 * 1024;
constexpr int ST_OFF = 4 << 20;

#define MEMBAR() asm volatile("" ::: "memory")
#define SCHED() __builtin_amdgcn_sched_barrier(0)

__device__ __forceinline__ float asf(u32 x) { return __builtin_bit_cast(float, x); }
__device__ __forceinline__ u32 asu(float x) { return __builtin_bit_cast(u32, x); }
__device__ __forceinline__ u32 pk_bf16(float lo, float hi) {
  f32x2 f = {lo, hi};
  return __builtin_bit_cast(u32, __builtin_convertvector(f, bf16x2));
}
__device__ __forceinline__ float bf_lo(u32 x) { return asf(x << 16); }
__device__ __forceinline__ float bf_hi(u32 x) { return asf(x & 0xFFFF0000u); }
__device__ __forceinline__ u32 clean(u32 x) { return x == SENT ? 0x7FC07FC0u : x; }
__device__ __forceinline__ float dot2(u32 a, u32 b, float c) {
  return __builtin_amdgcn_fdot2_f32_bf16(__builtin_bit_cast(bf16x2, a), __builtin_bit_cast(bf16x2, b), c, false);
}
template <int CTRL> __device__ __forceinline__ u32 dppu(u32 x) {
  return (u32)__builtin_amdgcn_update_dpp(0, (int)x, CTRL, 0xf, 0xf, true);
}
template <int CTRL> __device__ __forceinline__ float dppf(float x) { return asf(dppu<CTRL>(asu(x))); }
constexpr int DPP_X1 = 0xB1, DPP_X2 = 0x4E, DPP_HM = 0x141, DPP_R8 = 0x128, DPP_SHL4 = 0x104;
__device__ __forceinline__ float sum8(float x) {
  x += dppf<DPP_X1>(x); x += dppf<DPP_X2>(x); x += dppf<DPP_HM>(x); return x;
}
__device__ __forceinline__ float max8(float x) {
  x = fmaxf(x, dppf<DPP_X1>(x)); x = fmaxf(x, dppf<DPP_X2>(x)); x = fmaxf(x, dppf<DPP_HM>(x)); return x;
}
__device__ __forceinline__ float fold32(float a, float b) {
  auto r = __builtin_amdgcn_permlane32_swap(asu(a), asu(b), false, false);
  return asf(r[0]) + asf(r[1]);
}
__device__ __forceinline__ float fold16(float a, float b) {
  auto r = __builtin_amdgcn_permlane16_swap(asu(a), asu(b), false, false);
  return asf(r[0]) + asf(r[1]);
}
__device__ __forceinline__ float max32(float a) {
  auto r = __builtin_amdgcn_permlane32_swap(asu(a), asu(a), false, false);
  return fmaxf(asf(r[0]), asf(r[1]));
}
__device__ __forceinline__ float max16(float a) {
  auto r = __builtin_amdgcn_permlane16_swap(asu(a), asu(a), false, false);
  return fmaxf(asf(r[0]), asf(r[1]));
}
__device__ __forceinline__ float sum_hi(float x) {
  x += dppf<DPP_R8>(x); x = fold16(x, x); x = fold32(x, x); return x;
}
__device__ __forceinline__ float fold8(const float (&a)[8], int lane) {
  float b0 = fold32(a[0], a[4]), b1 = fold32(a[1], a[5]), b2 = fold32(a[2], a[6]), b3 = fold32(a[3], a[7]);
  float c0 = fold16(b0, b2), c1 = fold16(b1, b3);
  c0 += dppf<DPP_R8>(c0); c1 += dppf<DPP_R8>(c1);
  return (lane & 8) ? c1 : c0;
}
__device__ __forceinline__ u32 perm_lo(u32 r0, u32 r1) { return __builtin_amdgcn_perm(r1, r0, 0x05040100u); }
__device__ __forceinline__ u32 perm_hi(u32 r0, u32 r1) { return __builtin_amdgcn_perm(r1, r0, 0x07060302u); }
__device__ __forceinline__ void transpose8(const v4u (&r)[8], v4u (&t)[8]) {
#pragma unroll
  for (int c = 0; c < 4; ++c)
#pragma unroll
    for (int m = 0; m < 4; ++m) {
      t[2 * c][m] = perm_lo(r[2 * m][c], r[2 * m + 1][c]);
      t[2 * c + 1][m] = perm_hi(r[2 * m][c], r[2 * m + 1][c]);
    }
}
__device__ __forceinline__ bool ok4(v4u v) { return (v.x != SENT) & (v.y != SENT) & (v.z != SENT) & (v.w != SENT); }
__device__ __forceinline__ bool ok2(v2u v) { return (v.x != SENT) & (v.y != SENT); }

#define LD4(off) __builtin_amdgcn_raw_buffer_load_b128(rs, (off), 0, 16)
#define LD2(off) __builtin_amdgcn_raw_buffer_load_b64(rs, (off), 0, 16)
#define ST4(v, off) __builtin_amdgcn_raw_buffer_store_b128((v), rs, (off), 0, 16)
#define ST2(v, off) __builtin_amdgcn_raw_buffer_store_b64((v), rs, (off), 0, 16)

#define STAMP(n) do { } while (0)

struct __attribute__((aligned(16))) Lds {
  float ob[8][512];
  f32x2 ml[8][4];
  float rq[8][64];
  float rc[8][64];
  float ro[8][64];
  float rh[8][32];
  float c1[8][32];
  float rx[8][8];
  f32x2 sq[8];
  f32x2 sh[8];
  float xres[8];
  u32 q1[256];
};

__device__ __forceinline__ f32x2 wave_stats(const float (&x)[8]) {
  float s = ((x[0] + x[1]) + (x[2] + x[3])) + ((x[4] + x[5]) + (x[6] + x[7]));
  float mw = sum_hi(s) * (1.0f / 64.0f);
  float q = 0.f;
#pragma unroll
  for (int i = 0; i < 8; ++i) { float d = x[i] - mw; q = fmaf(d, d, q); }
  q = sum_hi(q);
  f32x2 r = {mw, q};
  return r;
}
__device__ __forceinline__ f32x2 row_stats(f32x2 st) {
  float mean = sum8(st.x) * 0.125f;
  float d = st.x - mean;
  float m2 = sum8(fmaf(64.0f * d, d, st.y));
  f32x2 r = {mean, m2 * (1.0f / 512.0f)};
  return r;
}

__global__ void __launch_bounds__(512, 2)
fused_decode(const u16* __restrict__ x_in, const u16* __restrict__ kc, const u16* __restrict__ vc,
             const u16* __restrict__ wq, const u16* __restrict__ wo, const u16* __restrict__ w1,
             const u16* __restrict__ b1, const u16* __restrict__ w2, const u16* __restrict__ b2,
             u16* __restrict__ out, unsigned char* ws) {
  __shared__ Lds L;
  STAMP(0);
  const int tid = threadIdx.x, lane = tid & 63;
  const int w = __builtin_amdgcn_readfirstlane(tid >> 6);
  const int hkv = blockIdx.x & 7, b = blockIdx.x >> 6, j = (blockIdx.x >> 3) & 7;
  const int ci = hkv * 8 + j;
  const int grp = b * 8 + hkv;
  const int p = lane & 7, rg = lane >> 3, c16 = lane & 15, g = lane >> 4;
  const __amdgpu_buffer_rsrc_t rs = __builtin_amdgcn_make_buffer_rsrc(ws, 0, WS_BYTES, 0x00020000);

  {
    int off = -1;
    if (tid < 256) off = AO_OFF + (((tid >> 7) * 32 + grp) * 8 + j) * 2048 + (tid & 127) * 16;
    else if (tid < 288) off = WO_OFF + ((((tid >> 4) & 1) * 4 + b) * 8 + hkv) * 2048 + j * 256 + (tid & 15) * 16;
    else if (tid < 296) off = Q2_OFF + grp * 1024 + j * 128 + (tid & 7) * 16;
    else if (tid < 304) off = H_OFF + (((tid >> 2) & 1) * 4 + b) * 4096 + ci * 64 + (tid & 3) * 16;
    else if (tid < 308) off = AS_OFF + (((tid >> 1) & 1) * 32 + grp) * 256 + j * 32 + (tid & 1) * 16;
    else if (tid == 308) off = X2_OFF + b * 1024 + ci * 16;
    if (off >= 0) { v4u s = {SENT, SENT, SENT, SENT}; ST4(s, off); }
  }
  MEMBAR();
  const v2u ep = LD2(EP_OFF + b * 256);
  const v4u xin = *(const v4u*)(x_in + b * D + 64 * w + 8 * rg);
  v4u tq[8], kf[8], tv[8], to[8];
  v2u t1[8];
  v2u t2[8];
  {
    v4u rq[8], rv[8];
    const u16* pq = wq + (size_t)(64 * w + 8 * rg) * NQ + hkv * 512 + 64 * j + 8 * p;
#pragma unroll
    for (int i = 0; i < 8; ++i) rq[i] = *(const v4u*)(pq + (size_t)i * NQ);
    const size_t kvbase = ((size_t)grp * SK + (SK - WIN) + 256 * j + 32 * w) * DH;
    const u16* pk = kc + kvbase + (8 * (c16 >> 2) + (c16 & 3)) * DH + 8 * g;
#pragma unroll
    for (int i = 0; i < 8; ++i) kf[i] = __builtin_nontemporal_load((const v4u*)(pk + (i >> 2) * 4 * DH + (i & 3) * 32));
    const u16* pv = vc + kvbase + (8 * g) * DH + 8 * c16;
#pragma unroll
    for (int i = 0; i < 8; ++i) rv[i] = __builtin_nontemporal_load((const v4u*)(pv + i * DH));
    MEMBAR(); SCHED();
    transpose8(rq, tq);
    STAMP(1);

    {
      float a[8], c[8], xf[8];
#pragma unroll
      for (int n = 0; n < 8; ++n) {
        a[n] = 0.f; c[n] = 0.f;
#pragma unroll
        for (int m = 0; m < 4; ++m) { a[n] = dot2(tq[n][m], xin[m], a[n]); c[n] = dot2(tq[n][m], 0x3F803F80u, c[n]); }
      }
#pragma unroll
      for (int m = 0; m < 4; ++m) { xf[2 * m] = bf_lo(xin[m]); xf[2 * m + 1] = bf_hi(xin[m]); }
      L.rq[w][8 * p + rg] = fold8(a, lane);
      L.rc[w][8 * p + rg] = fold8(c, lane);
      f32x2 st = wave_stats(xf);
      if (lane == 0) L.sq[w] = st;
    }
    __syncthreads();
    float csq = 0.f;
    const u32 tag_lo = (ep.x + 1u) & 0xFFFFu, tag_hi = ep.y + (tag_lo == 0u ? 1u : 0u);
    if (w == 0) {
      float s = 0.f;
#pragma unroll
      for (int u = 0; u < 8; ++u) { s += L.rq[u][lane]; csq += L.rc[u][lane]; }
      f32x2 mv = row_stats(L.sq[p]);
      float qv = (s - mv.x * csq) * __builtin_amdgcn_rsqf(mv.y + EPS);
      u32 qb = (pk_bf16(qv, qv) << 16) | tag_lo;
      u32 nb = dppu<DPP_X1>(qb);
      v4u gr = {qb, tag_hi, nb, tag_hi};
      if ((lane & 1) == 0) ST4(gr, Q1_OFF + grp * 4096 + (64 * j + lane) * 8);
    }
    STAMP(2);
    asm volatile("" : "+v"(kf[0]), "+v"(kf[1]), "+v"(kf[2]), "+v"(kf[3]), "+v"(kf[4]), "+v"(kf[5]), "+v"(kf[6]), "+v"(kf[7]) :: "memory");
    STAMP(3);
    const int qoff = tid < 256 ? Q1_OFF + grp * 4096 + tid * 16
                               : Q1_OFF + (b * 8 + ((tid >> 3) & 7)) * 4096 + (tid & 7) * 512;
    v4u qg = {0, 0, 0, 0};
    if (w < 5) qg = LD4(qoff);
    MEMBAR(); SCHED();
    v4u ro[8]; v2u r1[8]; v4u r2[4];
    {
      const u16* po = wo + (size_t)(hkv * 512 + 128 * (w >> 1) + 32 * (2 * (w & 1) + (rg >> 2)) + 2 * (rg & 3)) * D + 64 * j + 8 * p;
#pragma unroll
      for (int i = 0; i < 8; ++i) ro[i] = *(const v4u*)(po + (8 * (i & 3) + (i >> 2)) * D);
      const u16* p1 = w1 + (size_t)(64 * w + 8 * rg) * F + 32 * (ci < 56 ? ci : 55) + 4 * p;
#pragma unroll
      for (int i = 0; i < 8; ++i) r1[i] = *(const v2u*)(p1 + i * F);
      const u16* p2 = w2 + (size_t)(256 * (w < 7 ? w : 6) + 4 * lane) * D + 8 * ci;
#pragma unroll
      for (int i = 0; i < 4; ++i) r2[i] = *(const v4u*)(p2 + i * D);
    }
    const float b1v = bf_lo(b1[32 * (ci < 56 ? ci : 55) + (lane & 31)]);
    const float b2v = bf_lo(b2[8 * ci + p]);
    MEMBAR(); SCHED();
    if (w < 5) {
#define QOK() (((qg.x & 0xFFFFu) == tag_lo) & (qg.y == tag_hi) & ((qg.z & 0xFFFFu) == tag_lo) & (qg.w == tag_hi))
      if (__builtin_expect(!__all(QOK()), 0)) {
        for (u32 spins = 0; spins < SPIN_CAP; ++spins) {
          MEMBAR();
          qg = LD4(qoff);
          if (__all(QOK())) break;
        }
      }
      if (w < 4) L.q1[tid] = (qg.x >> 16) | (qg.z & 0xFFFF0000u);
    }
    __syncthreads();
    STAMP(4);
    transpose8(rv, tv);

    float xr[8];
#pragma unroll
    for (int m = 0; m < 4; ++m) { xr[2 * m] = bf_lo(xin[m]); xr[2 * m + 1] = bf_hi(xin[m]); }
    float cs1 = 0.f;

#pragma unroll
    for (int ly = 0; ly < 2; ++ly) {
      v4u qf[4];
      if (ly == 0) {
#pragma unroll
        for (int s = 0; s < 4; ++s) qf[s] = *(const v4u*)&L.q1[(c16 & 3) * 64 + 16 * s + 4 * g];
      } else {
        const int off = Q2_OFF + grp * 1024 + (c16 & 3) * 256 + 16 * g;
        for (u32 spins = 0; spins < SPIN_CAP; ++spins) {
          MEMBAR();
#pragma unroll
          for (int s = 0; s < 4; ++s) qf[s] = LD4(off + 64 * s);
          if (__all(ok4(qf[0]) & ok4(qf[1]) & ok4(qf[2]) & ok4(qf[3]))) break;
        }
      }
      STAMP(5 + 9 * ly);
      {
        f32x4 s0 = {0.f, 0.f, 0.f, 0.f}, s1 = {0.f, 0.f, 0.f, 0.f};
#pragma unroll
        for (int s = 0; s < 4; ++s) {
          s0 = __builtin_amdgcn_mfma_f32_16x16x32_bf16(__builtin_bit_cast(bf16x8, kf[s]), __builtin_bit_cast(bf16x8, qf[s]), s0, 0, 0, 0);
          s1 = __builtin_amdgcn_mfma_f32_16x16x32_bf16(__builtin_bit_cast(bf16x8, kf[4 + s]), __builtin_bit_cast(bf16x8, qf[s]), s1, 0, 0, 0);
        }
        const float sc = 0.08838834764831845f * 1.4426950408889634f;
        float m = fmaxf(fmaxf(fmaxf(s0[0], s0[1]), fmaxf(s0[2], s0[3])), fmaxf(fmaxf(s1[0], s1[1]), fmaxf(s1[2], s1[3])));
        m = max32(max16(m)) * sc;
        float e[8];
#pragma unroll
        for (int r = 0; r < 4; ++r) {
          e[r] = __builtin_amdgcn_exp2f(fmaf(s0[r], sc, -m));
          e[4 + r] = __builtin_amdgcn_exp2f(fmaf(s1[r], sc, -m));
        }
        v4u pf = {pk_bf16(e[0], e[1]), pk_bf16(e[2], e[3]), pk_bf16(e[4], e[5]), pk_bf16(e[6], e[7])};
        f32x4 o[8];
#pragma unroll
        for (int d = 0; d < 8; ++d) {
          f32x4 z = {0.f, 0.f, 0.f, 0.f};
          o[d] = __builtin_amdgcn_mfma_f32_16x16x32_bf16(__builtin_bit_cast(bf16x8, tv[d]), __builtin_bit_cast(bf16x8, pf), z, 0, 0, 0);
        }
        float ls = 0.f;
#pragma unroll
        for (int r = 0; r < 4; ++r) ls += bf_lo(pf[r]) + bf_hi(pf[r]);
        ls = fold16(ls, ls); ls = fold32(ls, ls);
        if (c16 < 4) {
#pragma unroll
          for (int d = 0; d < 8; ++d) *(f32x4*)&L.ob[w][((c16 * 4 + g) * 8 + d) * 4] = o[d];
          if (g == 0) { f32x2 t = {m, ls}; L.ml[w][c16] = t; }
        }
      }
      __syncthreads();
      {
        f32x2 t = L.ml[p][w >> 1];
        float ov[8];
#pragma unroll
        for (int u = 0; u < 8; ++u) ov[u] = L.ob[u][tid];
        float mx = max8(t.x);
        float e = __builtin_amdgcn_exp2f(t.x - mx);
        float lt = sum8(e * t.y);
        float o = 0.f;
#pragma unroll
        for (int u = 0; u < 8; ++u) o = fmaf(asf(__builtin_amdgcn_readlane(asu(e), u)), ov[u], o);
        u32 o0 = clean(asu(o));
        v4u v = {o0, dppu<0x55>(o0), dppu<0xAA>(o0), dppu<0xFF>(o0)};
        if ((lane & 3) == 0) ST4(v, AO_OFF + ((ly * 32 + grp) * 8 + j) * 2048 + tid * 4);
        if (lane == 1 && (w & 1) == 0) {
          v2u sv = {clean(asu(mx)), clean(asu(lt))};
          ST2(sv, AS_OFF + (ly * 32 + grp) * 256 + j * 32 + (w >> 1) * 8);
        }
      }
      STAMP(6 + 9 * ly);
      if (ly == 0) {
        transpose8(ro, to);
#pragma unroll
        for (int c = 0; c < 2; ++c)
#pragma unroll
          for (int m = 0; m < 4; ++m) {
            t1[4 * c + (m >> 1)][m & 1] = perm_lo(r1[2 * m][c], r1[2 * m + 1][c]);
            t1[4 * c + 2 + (m >> 1)][m & 1] = perm_hi(r1[2 * m][c], r1[2 * m + 1][c]);
          }
#pragma unroll
        for (int c = 0; c < 4; ++c)
#pragma unroll
          for (int m = 0; m < 2; ++m) {
            t2[2 * c][m] = perm_lo(r2[2 * m][c], r2[2 * m + 1][c]);
            t2[2 * c + 1][m] = perm_hi(r2[2 * m][c], r2[2 * m + 1][c]);
          }
        float c[4];
#pragma unroll
        for (int n = 0; n < 4; ++n) {
          c[n] = 0.f;
#pragma unroll
          for (int m = 0; m < 4; ++m) c[n] = dot2(t1[2 * n + (m >> 1)][m & 1], 0x3F803F80u, c[n]);
        }
        float v = fold16(fold32(c[0], c[2]), fold32(c[1], c[3]));
        v += dppf<DPP_R8>(v);
        if ((lane & 8) == 0) L.c1[w][4 * p + (lane >> 4)] = v;
      }
      {
        const int off = AO_OFF + ((ly * 32 + grp) * 8 + p) * 2048 + (64 * w + 8 * rg) * 4;
        const int soff = AS_OFF + (ly * 32 + grp) * 256 + p * 32 + (w >> 1) * 8;
        v4u a0, a1; v2u sv;
        for (u32 spins = 0; spins < SPIN_CAP; ++spins) {
          MEMBAR();
          sv = LD2(soff); a0 = LD4(off); a1 = LD4(off + 16);
          if (__all(ok4(a0) & ok4(a1) & ok2(sv))) break;
        }
        STAMP(7 + 9 * ly);
        float mx = max8(asf(sv.x));
        float e = __builtin_amdgcn_exp2f(asf(sv.x) - mx);
        float lt = sum8(e * asf(sv.y));
        float sc = e * __builtin_amdgcn_rcpf(lt);
        u32 ob[4];
#pragma unroll
        for (int m = 0; m < 2; ++m) {
          ob[m] = pk_bf16(sum8(asf(a0[2 * m]) * sc), sum8(asf(a0[2 * m + 1]) * sc));
          ob[2 + m] = pk_bf16(sum8(asf(a1[2 * m]) * sc), sum8(asf(a1[2 * m + 1]) * sc));
        }
        float a[8];
#pragma unroll
        for (int n = 0; n < 8; ++n) {
          a[n] = 0.f;
#pragma unroll
          for (int m = 0; m < 4; ++m) a[n] = dot2(to[n][m], ob[m], a[n]);
        }
        L.ro[w][8 * p + rg] = fold8(a, lane);
      }
      __syncthreads();
      if (w == 0) {
        float s = 0.f;
#pragma unroll
        for (int u = 0; u < 8; ++u) s += L.ro[u][lane];
        u32 o0 = clean(asu(s));
        v4u v = {o0, dppu<0x55>(o0), dppu<0xAA>(o0), dppu<0xFF>(o0)};
        if ((lane & 3) == 0) ST4(v, WO_OFF + ((ly * 4 + b) * 8 + hkv) * 2048 + (64 * j + lane) * 4);
        if (ly == 0) {
#pragma unroll
          for (int u = 0; u < 8; ++u) cs1 += L.c1[u][lane & 31];
        }
      }
      STAMP(8 + 9 * ly);
      {
        const int off = WO_OFF + ((ly * 4 + b) * 8 + p) * 2048 + (64 * w + 8 * rg) * 4;
        v4u a0, a1;
        for (u32 spins = 0; spins < SPIN_CAP; ++spins) {
          MEMBAR();
          a0 = LD4(off); a1 = LD4(off + 16);
          if (__all(ok4(a0) & ok4(a1))) break;
        }
        STAMP(9 + 9 * ly);
        u32 xb[4];
#pragma unroll
        for (int m = 0; m < 2; ++m) {
          xb[m] = pk_bf16(xr[2 * m] + sum8(asf(a0[2 * m])), xr[2 * m + 1] + sum8(asf(a0[2 * m + 1])));
          xb[2 + m] = pk_bf16(xr[4 + 2 * m] + sum8(asf(a1[2 * m])), xr[5 + 2 * m] + sum8(asf(a1[2 * m + 1])));
        }
        float a[4];
#pragma unroll
        for (int n = 0; n < 4; ++n) {
          a[n] = 0.f;
#pragma unroll
          for (int m = 0; m < 4; ++m) a[n] = dot2(t1[2 * n + (m >> 1)][m & 1], xb[m], a[n]);
        }
        float v = fold16(fold32(a[0], a[2]), fold32(a[1], a[3]));
        v += dppf<DPP_R8>(v);
        if ((lane & 8) == 0) L.rh[w][4 * p + (lane >> 4)] = v;
        float xf[8];
#pragma unroll
        for (int m = 0; m < 4; ++m) { xf[2 * m] = bf_lo(xb[m]); xf[2 * m + 1] = bf_hi(xb[m]); }
        f32x2 st = wave_stats(xf);
        if (lane == 0) L.sh[w] = st;
        if (w == hkv && lane == 8 * j) {
          f32x4 u0 = {xf[0], xf[1], xf[2], xf[3]}, u1 = {xf[4], xf[5], xf[6], xf[7]};
          *(f32x4*)&L.xres[0] = u0; *(f32x4*)&L.xres[4] = u1;
        }
      }
      __syncthreads();
      if (w == 0) {
        float s = 0.f;
#pragma unroll
        for (int u = 0; u < 8; ++u) s += L.rh[u][lane & 31];
        f32x2 mv = row_stats(L.sh[p]);
        float r1s = __builtin_amdgcn_rsqf(mv.y + EPS);
        float r2s = __builtin_amdgcn_rsqf(mv.y * r1s * r1s + EPS);
        float z = fmaf((s - mv.x * cs1), r1s * r2s, b1v);
        float u2 = z * fmaf(z * z, 0.044715f, 1.0f) * (-2.0f * 0.7978845608028654f * 1.4426950408889634f);
        float hv = z * __builtin_amdgcn_rcpf(1.0f + __builtin_amdgcn_exp2f(u2));
        u32 h0 = clean(pk_bf16(hv, dppf<DPP_X1>(hv)));
        u32 h1 = dppu<0xAA>(h0);
        v4u v = {h0, h1, dppu<DPP_SHL4>(h0), dppu<DPP_SHL4>(h1)};
        if (lane < 32 && (lane & 7) == 0 && ci < 56) ST4(v, H_OFF + (ly * 4 + b) * 4096 + ci * 64 + lane * 2);
      }
      STAMP(10 + 9 * ly);
      if (w < 7) {
        const int off = H_OFF + (ly * 4 + b) * 4096 + (256 * w + 4 * lane) * 2;
        v2u hv;
        for (u32 spins = 0; spins < SPIN_CAP; ++spins) {
          MEMBAR();
          hv = LD2(off);
          if (__all(ok2(hv))) break;
        }
        STAMP(11 + 9 * ly);
        float a[8];
#pragma unroll
        for (int n = 0; n < 8; ++n) a[n] = dot2(t2[n][1], hv.y, dot2(t2[n][0], hv.x, 0.f));
        float v = sum8(fold8(a, lane));
        if (p == 0) L.rx[w][rg] = v;
      }
      __syncthreads();
      if (w == 0) {
        float s = L.xres[p] + b2v;
#pragma unroll
        for (int u = 0; u < 7; ++u) s += L.rx[u][p];
        u32 h0 = pk_bf16(s, dppf<DPP_X1>(s));
        if (ly == 0) h0 = clean(h0);
        u32 h1 = dppu<0xAA>(h0);
        v4u v = {h0, h1, dppu<DPP_SHL4>(h0), dppu<DPP_SHL4>(h1)};
        if (lane == 0) {
          if (ly == 0) ST4(v, X2_OFF + b * 1024 + ci * 16);
          else {
            *(v4u*)(out + b * D + 8 * ci) = v;
            if (ci == 0) { v2u e2 = {tag_lo, tag_hi}; ST2(e2, EP_OFF + b * 256); }
          }
        }
      }
      STAMP(12 + 9 * ly);
      if (ly == 0) {
        const int off = X2_OFF + b * 1024 + (64 * w + 8 * rg) * 2;
        v4u xb;
        for (u32 spins = 0; spins < SPIN_CAP; ++spins) {
          MEMBAR();
          xb = LD4(off);
          if (__all(ok4(xb))) break;
        }
        STAMP(13);
        float a[8];
#pragma unroll
        for (int n = 0; n < 8; ++n) {
          a[n] = 0.f;
#pragma unroll
          for (int m = 0; m < 4; ++m) a[n] = dot2(tq[n][m], xb[m], a[n]);
        }
        L.rq[w][8 * p + rg] = fold8(a, lane);
#pragma unroll
        for (int m = 0; m < 4; ++m) { xr[2 * m] = bf_lo(xb[m]); xr[2 * m + 1] = bf_hi(xb[m]); }
        f32x2 st = wave_stats(xr);
        if (lane == 0) L.sq[w] = st;
        __syncthreads();
        if (w == 0) {
          float s = 0.f;
#pragma unroll
          for (int u = 0; u < 8; ++u) s += L.rq[u][lane];
          f32x2 mv = row_stats(L.sq[p]);
          float qv = (s - mv.x * csq) * __builtin_amdgcn_rsqf(mv.y + EPS);
          u32 h0 = clean(pk_bf16(qv, dppf<DPP_X1>(qv)));
          u32 h1 = dppu<0xAA>(h0);
          v4u v = {h0, h1, dppu<DPP_SHL4>(h0), dppu<DPP_SHL4>(h1)};
          if ((lane & 7) == 0) ST4(v, Q2_OFF + grp * 1024 + (64 * j + lane) * 2);
        }
        STAMP(22);
      }
    }
  }
}

extern "C" void kernel_launch(void* const* d_in, const int* in_sizes, int n_in, void* d_out, int out_size,
                              void* d_ws, size_t ws_size, hipStream_t stream) {
  fused_decode<<<256, 512, 0, stream>>>((const u16*)d_in[0], (const u16*)d_in[1], (const u16*)d_in[2],
                                        (const u16*)d_in[3], (const u16*)d_in[4], (const u16*)d_in[6],
                                        (const u16*)d_in[7], (const u16*)d_in[8], (const u16*)d_in[9],
                                        (u16*)d_out, (unsigned char*)d_ws);
}
```

```cpp
#include <hip/hip_runtime.h>
#include <stdint.h>

#define DIAG 0

typedef unsigned u32;
typedef unsigned long long u64;
typedef unsigned short u16;
typedef u32 v2u __attribute__((ext_vector_type(2)));
typedef u32 v4u __attribute__((ext_vector_type(4)));
typedef float f32x2 __attribute__((ext_vector_type(2)));
typedef float f32x4 __attribute__((ext_vector_type(4)));
typedef __bf16 bf16x2 __attribute__((ext_vector_type(2)));
typedef __bf16 bf16x8 __attribute__((ext_vector_type(8)));

constexpr int D = 512, HKV = 8, SK = 8192, DH = 128, WIN = 2048, NQ = 4096, F = 1792;
constexpr u32 SENT = 0xFFFFFFFFu;
constexpr u32 SPIN_CAP = 1u << 18;
constexpr float EPS = 1e-5f;

constexpr int EP_OFF = 0;
constexpr int Q1_OFF = 1024;
constexpr int Q2_OFF = Q1_OFF + 32 * 4096;
constexpr int AO_ST = 1152;
constexpr int AO_OFF = Q2_OFF + 32 * 1024;
constexpr int WO_OFF = AO_OFF + 2 * 32 * 8 * AO_ST;
constexpr int H_OFF = WO_OFF + 2 * 4 * 8 * 1024;
constexpr int X2_OFF = H_OFF + 2 * 4 * 4096;
constexpr int HT_OFF = X2_OFF + 2 * 4 * 8 * 1024;
constexpr int WS_BYTES = HT_OFF + 4 * 8 * 512;
constexpr int ST_OFF = 4 << 20;

#define PAR(i, v) (v)
#define P_AO0 PAR(0, 40)
#define P_WO0 PAR(1, 10)
#define P_H0 PAR(2, 7)
#define P_X PAR(3, 11)
#define P_Q PAR(4, 8)
#define P_AO1 PAR(5, 7)
#define P_WO1 PAR(6, 13)
#define P_H1 PAR(7, 7)
#define P_D PAR(8, 5)
#define P_R PAR(9, 2)
#define P_R0 PAR(10, 3)
#define P_F PAR(11, 7)

#define NK0 2
#define NW1 4
#define NW2 0
#define VBAR 1
#define MEMBAR() asm volatile("" ::: "memory")
#define SCHED() __builtin_amdgcn_sched_barrier(0)

__device__ __forceinline__ float asf(u32 x) { return __builtin_bit_cast(float, x); }
__device__ __forceinline__ u32 asu(float x) { return __builtin_bit_cast(u32, x); }
__device__ __forceinline__ u32 pk_bf16(float lo, float hi) {
  f32x2 f = {lo, hi};
  return __builtin_bit_cast(u32, __builtin_convertvector(f, bf16x2));
}
__device__ __forceinline__ float bf_lo(u32 x) { return asf(x << 16); }
__device__ __forceinline__ float bf_hi(u32 x) { return asf(x & 0xFFFF0000u); }
__device__ __forceinline__ u32 clean(u32 x) { return x == SENT ? 0x7FC07FC0u : x; }
__device__ __forceinline__ float dot2(u32 a, u32 b, float c) {
  return __builtin_amdgcn_fdot2_f32_bf16(__builtin_bit_cast(bf16x2, a), __builtin_bit_cast(bf16x2, b), c, false);
}
template <int CTRL> __device__ __forceinline__ u32 dppu(u32 x) {
  return (u32)__builtin_amdgcn_update_dpp(0, (int)x, CTRL, 0xf, 0xf, true);
}
template <int CTRL> __device__ __forceinline__ float dppf(float x) { return asf(dppu<CTRL>(asu(x))); }
constexpr int DPP_X1 = 0xB1, DPP_X2 = 0x4E, DPP_HM = 0x141, DPP_R8 = 0x128, DPP_SHL4 = 0x104;
__device__ __forceinline__ float sum8(float x) {
  x += dppf<DPP_X1>(x); x += dppf<DPP_X2>(x); x += dppf<DPP_HM>(x); return x;
}
__device__ __forceinline__ float vmax(float a, float b) { float r; asm("v_max_f32 %0, %1, %2" : "=v"(r) : "v"(a), "v"(b)); return r; }
__device__ __forceinline__ float max8(float x) {
  float r;
  asm("s_nop 1\n v_max_f32_dpp %0, %1, %1 quad_perm:[1,0,3,2] row_mask:0xf bank_mask:0xf\n"
      "s_nop 1\n v_max_f32_dpp %0, %0, %0 quad_perm:[2,3,0,1] row_mask:0xf bank_mask:0xf\n"
      "s_nop 1\n v_max_f32_dpp %0, %0, %0 row_half_mirror row_mask:0xf bank_mask:0xf" : "=&v"(r) : "v"(x));
  return r;
}
__device__ __forceinline__ float fold32(float a, float b) {
  auto r = __builtin_amdgcn_permlane32_swap(asu(a), asu(b), false, false);
  return asf(r[0]) + asf(r[1]);
}
__device__ __forceinline__ float fold16(float a, float b) {
  auto r = __builtin_amdgcn_permlane16_swap(asu(a), asu(b), false, false);
  return asf(r[0]) + asf(r[1]);
}
__device__ __forceinline__ float max32(float a) {
  auto r = __builtin_amdgcn_permlane32_swap(asu(a), asu(a), false, false);
  return vmax(asf(r[0]), asf(r[1]));
}
__device__ __forceinline__ float max16(float a) {
  auto r = __builtin_amdgcn_permlane16_swap(asu(a), asu(a), false, false);
  return vmax(asf(r[0]), asf(r[1]));
}
__device__ __forceinline__ float sum_hi(float x) {
  x += dppf<DPP_R8>(x); x = fold16(x, x); x = fold32(x, x); return x;
}
__device__ __forceinline__ float fold8(const float (&a)[8], int lane) {
  float b0 = fold32(a[0], a[4]), b1 = fold32(a[1], a[5]), b2 = fold32(a[2], a[6]), b3 = fold32(a[3], a[7]);
  float c0 = fold16(b0, b2), c1 = fold16(b1, b3);
  c0 += dppf<DPP_R8>(c0); c1 += dppf<DPP_R8>(c1);
  return (lane & 8) ? c1 : c0;
}
__device__ __forceinline__ u32 perm_lo(u32 r0, u32 r1) { return __builtin_amdgcn_perm(r1, r0, 0x05040100u); }
__device__ __forceinline__ u32 perm_hi(u32 r0, u32 r1) { return __builtin_amdgcn_perm(r1, r0, 0x07060302u); }
__device__ __forceinline__ void transpose8(const v4u (&r)[8], v4u (&t)[8]) {
#pragma unroll
  for (int c = 0; c < 4; ++c)
#pragma unroll
    for (int m = 0; m < 4; ++m) {
      t[2 * c][m] = perm_lo(r[2 * m][c], r[2 * m + 1][c]);
      t[2 * c + 1][m] = perm_hi(r[2 * m][c], r[2 * m + 1][c]);
    }
}
__device__ __forceinline__ bool ok4(v4u v) { return (v.x != SENT) & (v.y != SENT) & (v.z != SENT) & (v.w != SENT); }
__device__ __forceinline__ bool ok2(v2u v) { return (v.x != SENT) & (v.y != SENT); }

#define LD4(off) __builtin_amdgcn_raw_buffer_load_b128(rs, (off), 0, 16)
#define LD1(off) __builtin_amdgcn_raw_buffer_load_b32(rs, (off), 0, 16)
#define LD2(off) __builtin_amdgcn_raw_buffer_load_b64(rs, (off), 0, 16)
#define ST4(v, off) __builtin_amdgcn_raw_buffer_store_b128((v), rs, (off), 0, 16)
#define ST2(v, off) __builtin_amdgcn_raw_buffer_store_b64((v), rs, (off), 0, 16)

#define DW 0
#define STAMP(n) do { } while (0)

#define BAR() asm volatile("s_waitcnt lgkmcnt(0)\n s_barrier" ::: "memory")
#define NAP(n) do { _Pragma("nounroll") for (int i_ = (n); i_ > 0; --i_) __builtin_amdgcn_s_sleep(1); } while (0)
#define PIN8(a) asm volatile("" : "+v"(a[0]), "+v"(a[1]), "+v"(a[2]), "+v"(a[3]), "+v"(a[4]), "+v"(a[5]), "+v"(a[6]), "+v"(a[7]) :: "memory")
#define POLL(load, good, pause) do { MEMBAR(); load; \
  if (__builtin_expect(!__all(good), 0)) { _Pragma("nounroll") for (u32 spins = 0; spins < SPIN_CAP; ++spins) { \
    NAP(pause); MEMBAR(); if (!(good)) { load; } if (__all(good)) break; } } } while (0)
#define NTL4(p) __builtin_nontemporal_load((const v4u*)(p))
#define NTL2(p) __builtin_nontemporal_load((const v2u*)(p))
#define MFMA(a, b, c) __builtin_amdgcn_mfma_f32_16x16x32_bf16(__builtin_bit_cast(bf16x8, a), __builtin_bit_cast(bf16x8, b), c, 0, 0, 0)
constexpr u32 ONES = 0x3F803F80u;

struct __attribute__((aligned(16))) Lds {
  float ob[8][16 * 36];
  f32x2 ml[8][4];
  float rq[8][64];
  float rc[8][64];
  float ro[8][64];
  float rh[8][64];
  float c1[8][64];
  float rx[7][64];
  float sq[8][2];
  u32 q1[256];
};

__device__ __forceinline__ float wave_stats(u32 x0, u32 x1, u32 x2, u32 x3) {
  float s = dot2(x1, ONES, dot2(x0, ONES, 0.f)) + dot2(x3, ONES, dot2(x2, ONES, 0.f));
  float q = dot2(x1, x1, dot2(x0, x0, 0.f)) + dot2(x3, x3, dot2(x2, x2, 0.f));
  float t = fold32(s, q);
  t = fold16(t, t);
  return t + dppf<DPP_R8>(t);
}
__device__ __forceinline__ v4u pack8(float v) {
  u32 h0 = clean(pk_bf16(v, dppf<DPP_X1>(v)));
  u32 h1 = dppu<0xAA>(h0);
  v4u r = {h0, h1, dppu<DPP_SHL4>(h0), dppu<DPP_SHL4>(h1)};
  return r;
}

__global__ void __launch_bounds__(512, 2)
fused_decode(const u16* __restrict__ x_in, const u16* __restrict__ kc, const u16* __restrict__ vc,
             const u16* __restrict__ wq, const u16* __restrict__ wo, const u16* __restrict__ w1,
             const u16* __restrict__ b1, const u16* __restrict__ w2, const u16* __restrict__ b2,
             u16* __restrict__ out, unsigned char* ws) {
  __shared__ Lds L;
  STAMP(0);
  const int tid = threadIdx.x, lane = tid & 63;
  const int w = __builtin_amdgcn_readfirstlane(tid >> 6);
  const int hkv = blockIdx.x & 7, b = blockIdx.x >> 6, j = (blockIdx.x >> 3) & 7;
  const int jo = (j + 3) & 7, j2 = (j + 6) & 7;
  const int ci = hkv * 8 + j;
  const int grp = b * 8 + hkv;
  const int p = lane & 7, rg = lane >> 3, c16 = lane & 15, g = lane >> 4;
  const __amdgpu_buffer_rsrc_t rs = __builtin_amdgcn_make_buffer_rsrc(ws, 0, WS_BYTES, 0x00020000);

  {
    int off = -1;
    if (tid < 72) off = AO_OFF + (grp * 8 + j) * AO_ST + tid * 16;
    else if (tid < 144) off = AO_OFF + ((32 + grp) * 8 + j) * AO_ST + (tid - 72) * 16;
    else if (tid < 160) off = WO_OFF + ((((tid >> 3) & 1) * 4 + b) * 8 + hkv) * 1024 + jo * 128 + (tid & 7) * 16;
    else if (tid < 168) off = Q2_OFF + grp * 1024 + j * 128 + (tid & 7) * 16;
    else if (tid < 176) off = H_OFF + (((tid >> 2) & 1) * 4 + b) * 4096 + ci * 64 + (tid & 3) * 16;
    else if (tid < 192) off = X2_OFF + ((((tid >> 3) & 1) * 4 + b) * 8 + hkv) * 1024 + j2 * 128 + (tid & 7) * 16;
    if (off >= 0) { v4u s = {SENT, SENT, SENT, SENT}; ST4(s, off); }
  }
  MEMBAR();
  const v2u ep = LD2(EP_OFF + b * 256);
  const v4u xin = *(const v4u*)(x_in + b * D + 64 * w + 8 * rg);
  v4u tq[8], kf[8], tv[8], to[8];
  v2u t1[8];
  v2u t2[8];
  v4u rq[8], rv[8], ro[8], r2[4];
  v2u r1[8];
  {
    const u16* pq = wq + (size_t)(64 * w + 8 * rg) * NQ + hkv * 512 + 64 * j + 8 * p;
#pragma unroll
    for (int i = 0; i < 8; ++i) rq[i] = NTL4(pq + (size_t)i * NQ);
  }
  const int c1 = 224 * hkv + 32 * (j < 7 ? j : 6);
  const float b1v = bf_lo(b1[c1 + (lane & 31)]);
  const u32 b2r = b2[64 * j2 + lane];
  MEMBAR(); SCHED();
  __builtin_amdgcn_s_barrier();
  MEMBAR(); SCHED();
  const size_t kvbase = ((size_t)grp * SK + (SK - WIN) + 256 * j + 32 * w) * DH;
  const u16* pk = kc + kvbase + (8 * (c16 >> 2) + (c16 & 3)) * DH + 8 * g;
#pragma unroll
  for (int i = 0; i < NK0; ++i) kf[i] = NTL4(pk + (i >> 2) * 4 * DH + (i & 3) * 32);
  MEMBAR(); SCHED();
  transpose8(rq, tq);
  STAMP(1);

  {
    float a[8], c[8];
#pragma unroll
    for (int n = 0; n < 8; ++n) {
      a[n] = 0.f; c[n] = 0.f;
#pragma unroll
      for (int m = 0; m < 4; ++m) { a[n] = dot2(tq[n][m], xin[m], a[n]); c[n] = dot2(tq[n][m], ONES, c[n]); }
    }
    L.rq[w][8 * p + rg] = fold8(a, lane);
    L.rc[w][8 * p + rg] = fold8(c, lane);
    float st = wave_stats(xin.x, xin.y, xin.z, xin.w);
    if ((lane & 31) == 0) L.sq[w][lane >> 5] = st;
  }
  BAR();
  float csq = 0.f;
  const u32 tag_lo = (ep.x + 1u) & 0xFFFFu, tag_hi = ep.y + (tag_lo == 0u ? 1u : 0u);
  if (w == 0) {
    float s = 0.f;
#pragma unroll
    for (int u = 0; u < 8; ++u) { s += L.rq[u][lane]; csq += L.rc[u][lane]; }
    f32x2 t = *(const f32x2*)L.sq[p];
    float mean = sum8(t.x) * (1.0f / 512.0f);
    float var = vmax(fmaf(sum8(t.y), 1.0f / 512.0f, -mean * mean), 0.f);
    float qv = (s - mean * csq) * __builtin_amdgcn_rsqf(var + EPS);
    u32 qb = (pk_bf16(qv, qv) << 16) | tag_lo;
    u32 nb = dppu<DPP_X1>(qb);
    v4u gr = {qb, tag_hi, nb, tag_hi};
    if ((lane & 1) == 0) ST4(gr, Q1_OFF + grp * 4096 + (64 * j + lane) * 8);
  }
  STAMP(2);
  MEMBAR(); SCHED();
#define QOK(q) ((((q).x & 0xFFFFu) == tag_lo) & ((q).y == tag_hi) & (((q).z & 0xFFFFu) == tag_lo) & ((q).w == tag_hi))
  const int rh_idx = (lane & 8) ? 32 + (lane >> 5) : 4 * p + (lane >> 4);
  const bool rh_on = !(lane & 8) | ((lane & 31) == 8);
  const u16* p1 = w1 + (size_t)(64 * w + 8 * rg) * F + c1 + 4 * p;
  const u16* p2 = w2 + (size_t)(224 * hkv + 32 * (w < 7 ? w : 6) + 4 * rg) * D + 64 * j2 + 8 * p;
  {
    const u16* po = wo + (size_t)(hkv * 512 + 128 * (w >> 1) + 32 * (2 * (w & 1) + (rg >> 2)) + 2 * (rg & 3)) * D + 64 * jo + 8 * p;
    const u16* pv = vc + kvbase + (8 * g) * DH + 8 * c16;
#pragma unroll
    for (int i = NK0; i < 8; ++i) kf[i] = NTL4(pk + (i >> 2) * 4 * DH + (i & 3) * 32);
#pragma unroll
    for (int i = 0; i < 8; ++i) rv[i] = NTL4(pv + i * DH);
    MEMBAR(); SCHED();
    if (VBAR) __builtin_amdgcn_s_barrier();
    const int qoff = Q1_OFF + grp * 4096 + (tid & 255) * 16;
    v4u qg = LD4(qoff);
    MEMBAR(); SCHED();
#pragma unroll
    for (int i = 0; i < 8; ++i) ro[i] = NTL4(po + (8 * (i & 3) + (i >> 2)) * D);
#pragma unroll
    for (int i = 0; i < NW1; ++i) r1[i] = NTL2(p1 + i * F);
#pragma unroll
    for (int i = 0; i < NW2; ++i) r2[i] = NTL4(p2 + i * D);
    MEMBAR(); SCHED();
    STAMP(3);
    transpose8(rv, tv);
    PIN8(tv);
    if (w < 4) {
      if (__builtin_expect(!__all(QOK(qg)), 0)) {
        for (u32 spins = 0; spins < SPIN_CAP; ++spins) {
          NAP(P_R);
          MEMBAR();
          qg = LD4(qoff);
          if (__all(QOK(qg))) break;
        }
      }
      L.q1[tid] = (qg.x >> 16) | (qg.z & 0xFFFF0000u);
    }
  }
  BAR();
  STAMP(4);
  const u32 t_own = (u32)__builtin_amdgcn_s_memrealtime() << 16;
  if (w == 7 && lane < 8) { v2u t = {tag_lo | t_own, tag_hi}; ST2(t, HT_OFF + (b * 8 + lane) * 512 + ci * 8); }

  v4u xb = xin;
  float cs1 = 0.f;

#pragma unroll
  for (int ly = 0; ly < 2; ++ly) {
    v4u qf[4];
    if (ly == 0) {
      qf[0] = *(const v4u*)&L.q1[(c16 & 3) * 64 + (c16 >> 2) * 16 + 4 * g];
    } else {
      const int off = Q2_OFF + grp * 1024 + (c16 & 3) * 256 + (c16 >> 2) * 64 + 16 * g;
      NAP(P_Q);
      POLL(qf[0] = LD4(off), ok4(qf[0]), P_R);
    }
#pragma unroll
    for (int c = 0; c < 4; ++c) {
      qf[1][c] = dppu<0x104>(qf[0][c]); qf[2][c] = dppu<0x108>(qf[0][c]); qf[3][c] = dppu<0x10C>(qf[0][c]);
    }
    STAMP(5 + 9 * ly);
    {
      f32x4 s0 = {0.f, 0.f, 0.f, 0.f}, s1 = {0.f, 0.f, 0.f, 0.f};
#pragma unroll
      for (int s = 0; s < 4; ++s) { s0 = MFMA(kf[s], qf[s], s0); s1 = MFMA(kf[4 + s], qf[s], s1); }
      const float sc = 0.08838834764831845f * 1.4426950408889634f;
      float m = fmaxf(fmaxf(fmaxf(fmaxf(s0[0], s0[1]), s0[2]), fmaxf(fmaxf(s0[3], s1[0]), s1[1])), fmaxf(s1[2], s1[3]));
      m = max32(max16(m)) * sc;
      float e[8];
#pragma unroll
      for (int r = 0; r < 4; ++r) {
        e[r] = __builtin_amdgcn_exp2f(fmaf(s0[r], sc, -m));
        e[4 + r] = __builtin_amdgcn_exp2f(fmaf(s1[r], sc, -m));
      }
      v4u pf = {pk_bf16(e[0], e[1]), pk_bf16(e[2], e[3]), pk_bf16(e[4], e[5]), pk_bf16(e[6], e[7])};
      const f32x4 z = {0.f, 0.f, 0.f, 0.f};
      const v4u ones = {ONES, ONES, ONES, ONES};
      f32x4 ls = MFMA(ones, pf, z);
      f32x4 o[8];
#pragma unroll
      for (int d = 0; d < 8; ++d) o[d] = MFMA(tv[d], pf, z);
      if (c16 < 4) {
        if (g == 0) { f32x2 t = {m, ls[0]}; L.ml[w][c16] = t; }
#pragma unroll
        for (int d = 0; d < 8; ++d) *(f32x4*)&L.ob[w][(c16 * 4 + g) * 36 + d * 4] = o[d];
      }
    }
    STAMP(23); BAR(); STAMP(24);
    {
      f32x2 t = L.ml[p][w >> 1];
      float ov[8];
#pragma unroll
      for (int u = 0; u < 8; ++u) ov[u] = L.ob[u][(tid >> 5) * 36 + (tid & 31)];
      float mx = max8(t.x);
      float e = __builtin_amdgcn_exp2f(t.x - mx);
      float lt = sum8(e * t.y);
      e *= __builtin_amdgcn_rcpf(lt);
      float o0 = 0.f, o1 = 0.f;
#pragma unroll
      for (int u = 0; u < 4; ++u) {
        o0 = fmaf(asf(__builtin_amdgcn_readlane(asu(e), u)), ov[u], o0);
        o1 = fmaf(asf(__builtin_amdgcn_readlane(asu(e), 4 + u)), ov[4 + u], o1);
      }
      v4u v = pack8(o0 + o1);
      const u32 lse = clean(asu(mx + __builtin_amdgcn_logf(lt)));
      if (lane == 1) v.x = lse;
      const int off = AO_OFF + ((ly * 32 + grp) * 8 + j) * AO_ST + (lane == 1 ? 1024 + 16 * w : tid * 2);
      if ((p == 0) | (lane == 1)) ST4(v, off);
    }
    STAMP(6 + 9 * ly);
    {
      u32 sv; v4u a0;
      const int off = AO_OFF + ((ly * 32 + grp) * 8 + p) * AO_ST;
      if (ly == 0) {
        MEMBAR(); SCHED();
#pragma unroll
        for (int i = NW1; i < 8; ++i) r1[i] = NTL2(p1 + i * F);
#pragma unroll
        for (int i = NW2; i < 4; ++i) r2[i] = NTL4(p2 + i * D);
        v2u t = LD2(HT_OFF + (b * 8 + hkv) * 512 + lane * 8);
        MEMBAR(); SCHED();
        PIN8(ro);
    transpose8(ro, to);
    PIN8(to);
#pragma unroll
        for (int c = 0; c < 2; ++c)
#pragma unroll
          for (int m = 0; m < 4; ++m) {
            t1[4 * c + (m >> 1)][m & 1] = perm_lo(r1[2 * m][c], r1[2 * m + 1][c]);
            t1[4 * c + 2 + (m >> 1)][m & 1] = perm_hi(r1[2 * m][c], r1[2 * m + 1][c]);
          }
        float c[4];
#pragma unroll
        for (int n = 0; n < 4; ++n) {
          c[n] = 0.f;
#pragma unroll
          for (int m = 0; m < 4; ++m) c[n] = dot2(t1[2 * n + (m >> 1)][m & 1], ONES, c[n]);
        }
        float v = fold16(fold32(c[0], c[2]), fold32(c[1], c[3]));
        v += dppf<DPP_R8>(v);
        if ((lane & 8) == 0) L.c1[w][rh_idx] = v;
#pragma unroll
        for (int c = 0; c < 4; ++c)
#pragma unroll
          for (int m = 0; m < 2; ++m) {
            t2[2 * c][m] = perm_lo(r2[2 * m][c], r2[2 * m + 1][c]);
            t2[2 * c + 1][m] = perm_hi(r2[2 * m][c], r2[2 * m + 1][c]);
          }
    PIN8(t2);
        if (w == 7) {
#define HOK() ((((t.x & 0xFFFFu) == tag_lo) & (t.y == tag_hi)) | (lane == ci))
          if (!__all(HOK())) {
            for (u32 spins = 0; spins < SPIN_CAP; ++spins) {
              NAP(P_R0);
              MEMBAR();
              if (!HOK()) t = LD2(HT_OFF + (b * 8 + hkv) * 512 + lane * 8);
              if (__all(HOK())) break;
            }
          }
          const u32 tt = lane == ci ? t_own : (t.x & 0xFFFF0000u);
          for (int n = 0; n < 100; ++n) {
            const u32 now = (u32)__builtin_amdgcn_s_memrealtime() << 16;
            if (__all(now - tt >= (u32)(P_AO0) << 18)) break;
            __builtin_amdgcn_s_sleep(1);
          }
        }
        BAR();
      } else NAP(P_AO1);
      POLL((sv = LD1(off + 1024 + 16 * w), a0 = LD4(off + (64 * w + 8 * rg) * 2)), ok4(a0) & (sv != SENT), P_R);
      STAMP(7 + 9 * ly);
      float e = __builtin_amdgcn_exp2f(asf(sv) - max8(asf(sv)));
      e *= __builtin_amdgcn_rcpf(sum8(e));
      u32 ob[4];
#pragma unroll
      for (int m = 0; m < 4; ++m) ob[m] = pk_bf16(sum8(bf_lo(a0[m]) * e), sum8(bf_hi(a0[m]) * e));
      float a[8];
#pragma unroll
      for (int n = 0; n < 8; ++n) {
        a[n] = 0.f;
#pragma unroll
        for (int m = 0; m < 4; ++m) a[n] = dot2(to[n][m], ob[m], a[n]);
      }
      L.ro[w][8 * p + rg] = fold8(a, lane);
    }
    STAMP(25); BAR(); STAMP(26);
    if (w == 0) {
      float u0 = L.ro[0][lane], u1 = L.ro[1][lane], u2 = L.ro[2][lane], u3 = L.ro[3][lane];
      float u4 = L.ro[4][lane], u5 = L.ro[5][lane], u6 = L.ro[6][lane], u7 = L.ro[7][lane];
      v4u v = pack8(((u0 + u1) + (u2 + u3)) + ((u4 + u5) + (u6 + u7)));
      if (p == 0) ST4(v, WO_OFF + ((ly * 4 + b) * 8 + hkv) * 1024 + (64 * jo + lane) * 2);
    } else NAP(P_D);
    STAMP(8 + 9 * ly);
    {
      v4u a0;
      const int off = WO_OFF + ((ly * 4 + b) * 8 + p) * 1024 + (64 * w + 8 * rg) * 2;
      NAP(ly == 0 ? P_WO0 : P_WO1);
      POLL(a0 = LD4(off), ok4(a0), P_R);
      STAMP(9 + 9 * ly);
#pragma unroll
      for (int m = 0; m < 4; ++m) xb[m] = pk_bf16(bf_lo(xb[m]) + sum8(bf_lo(a0[m])), bf_hi(xb[m]) + sum8(bf_hi(a0[m])));
      float a[4];
#pragma unroll
      for (int n = 0; n < 4; ++n) {
        a[n] = 0.f;
#pragma unroll
        for (int m = 0; m < 4; ++m) a[n] = dot2(t1[2 * n + (m >> 1)][m & 1], xb[m], a[n]);
      }
      float v = fold16(fold32(a[0], a[2]), fold32(a[1], a[3]));
      v += dppf<DPP_R8>(v);
      float st = wave_stats(xb.x, xb.y, xb.z, xb.w);
      if (rh_on) L.rh[w][rh_idx] = (lane & 8) ? st : v;
    }
    STAMP(27); BAR(); STAMP(28);
    if (w == 0) {
      float u0 = L.rh[0][lane], u1 = L.rh[1][lane], u2 = L.rh[2][lane], u3 = L.rh[3][lane];
      float u4 = L.rh[4][lane], u5 = L.rh[5][lane], u6 = L.rh[6][lane], u7 = L.rh[7][lane];
      if (ly == 0) {
#pragma unroll
        for (int u = 0; u < 8; ++u) cs1 += L.c1[u][lane];
      }
      float s = ((u0 + u1) + (u2 + u3)) + ((u4 + u5) + (u6 + u7));
      float mean = asf(__builtin_amdgcn_readlane(asu(s), 32)) * (1.0f / 512.0f);
      float var = vmax(fmaf(asf(__builtin_amdgcn_readlane(asu(s), 33)), 1.0f / 512.0f, -mean * mean), 0.f);
      float r1s = __builtin_amdgcn_rsqf(var + EPS);
      float r2s = __builtin_amdgcn_rsqf(var * r1s * r1s + EPS);
      float z = fmaf((s - mean * cs1), r1s * r2s, b1v);
      float u2e = z * fmaf(z * z, 0.044715f, 1.0f) * (-2.0f * 0.7978845608028654f * 1.4426950408889634f);
      float hv = z * __builtin_amdgcn_rcpf(1.0f + __builtin_amdgcn_exp2f(u2e));
      v4u v = pack8(hv);
      if (lane < 32 && p == 0 && j < 7) ST4(v, H_OFF + (ly * 4 + b) * 4096 + ci * 64 + lane * 2);
    } else NAP(P_D + 1);
    STAMP(10 + 9 * ly);
    if (w < 7) {
      const int off = H_OFF + (ly * 4 + b) * 4096 + hkv * 512 + w * 64 + rg * 8;
      NAP(ly == 0 ? P_H0 : P_H1);
      v2u hv;
      POLL(hv = LD2(off), ok2(hv), P_R);
      STAMP(11 + 9 * ly);
      float a[8];
#pragma unroll
      for (int n = 0; n < 8; ++n) a[n] = dot2(t2[n][1], hv.y, dot2(t2[n][0], hv.x, 0.f));
      L.rx[w][8 * p + rg] = fold8(a, lane);
    }
    STAMP(29); BAR();
    if (w == 0) {
      float u0 = L.rx[0][lane], u1 = L.rx[1][lane], u2 = L.rx[2][lane], u3 = L.rx[3][lane];
      float u4 = L.rx[4][lane], u5 = L.rx[5][lane], u6 = L.rx[6][lane];
      v4u v = pack8(((u0 + u1) + (u2 + u3)) + ((u4 + u5) + (u6 + (hkv == 0 ? bf_lo(b2r) : 0.f))));
      if (p == 0) ST4(v, X2_OFF + ((ly * 4 + b) * 8 + hkv) * 1024 + (64 * j2 + lane) * 2);
    } else NAP(P_D);
    if (ly == 0) STAMP(12);
    if (ly == 0 || (hkv == j && w == j)) {
      const int off = X2_OFF + ((ly * 4 + b) * 8 + p) * 1024 + (64 * w + 8 * rg) * 2;
      v4u a0;
      NAP(ly == 0 ? P_X : P_F);
      POLL(a0 = LD4(off), ok4(a0), P_R);
#pragma unroll
      for (int m = 0; m < 4; ++m) xb[m] = pk_bf16(bf_lo(xb[m]) + sum8(bf_lo(a0[m])), bf_hi(xb[m]) + sum8(bf_hi(a0[m])));
      if (ly == 1) {
        if (p == 0) *(v4u*)(out + b * D + 64 * w + 8 * rg) = xb;
        if (ci == 0 && lane == 0) { v2u e2 = {tag_lo, tag_hi}; ST2(e2, EP_OFF + b * 256); }
      }
    }
    if (ly == 1) STAMP(21);
    if (ly == 0) {
      STAMP(13);
      float a[8];
#pragma unroll
      for (int n = 0; n < 8; ++n) {
        a[n] = 0.f;
#pragma unroll
        for (int m = 0; m < 4; ++m) a[n] = dot2(tq[n][m], xb[m], a[n]);
      }
      L.rq[w][8 * p + rg] = fold8(a, lane);
      float st = wave_stats(xb.x, xb.y, xb.z, xb.w);
      if ((lane & 31) == 0) L.sq[w][lane >> 5] = st;
      STAMP(30); BAR(); STAMP(31);
      if (w == 0) {
        f32x2 t = *(const f32x2*)L.sq[p];
        float u0 = L.rq[0][lane], u1 = L.rq[1][lane], u2 = L.rq[2][lane], u3 = L.rq[3][lane];
        float u4 = L.rq[4][lane], u5 = L.rq[5][lane], u6 = L.rq[6][lane], u7 = L.rq[7][lane];
        float mean = sum8(t.x) * (1.0f / 512.0f);
        float var = vmax(fmaf(sum8(t.y), 1.0f / 512.0f, -mean * mean), 0.f);
        float s = ((u0 + u1) + (u2 + u3)) + ((u4 + u5) + (u6 + u7));
        v4u v = pack8((s - mean * csq) * __builtin_amdgcn_rsqf(var + EPS));
        if (p == 0) ST4(v, Q2_OFF + grp * 1024 + (64 * j + lane) * 2);
      } else NAP(P_D);
      STAMP(22);
    }
  }
}

extern "C" void kernel_launch(void* const* d_in, const int* in_sizes, int n_in, void* d_out, int out_size,
                              void* d_ws, size_t ws_size, hipStream_t stream) {
  fused_decode<<<256, 512, 0, stream>>>((const u16*)d_in[0], (const u16*)d_in[1], (const u16*)d_in[2],
                                        (const u16*)d_in[3], (const u16*)d_in[4], (const u16*)d_in[6],
                                        (const u16*)d_in[7], (const u16*)d_in[8], (const u16*)d_in[9],
                                        (u16*)d_out, (unsigned char*)d_ws);
}
```

```cpp
#include <hip/hip_runtime.h>
#include <stdint.h>

#define DIAG 0

typedef unsigned u32;
typedef unsigned long long u64;
typedef unsigned short u16;
typedef u32 v2u __attribute__((ext_vector_type(2)));
typedef u32 v4u __attribute__((ext_vector_type(4)));
typedef float f32x2 __attribute__((ext_vector_type(2)));
typedef float f32x4 __attribute__((ext_vector_type(4)));
typedef __bf16 bf16x2 __attribute__((ext_vector_type(2)));
typedef __bf16 bf16x8 __attribute__((ext_vector_type(8)));

constexpr int D = 512, HKV = 8, SK = 8192, DH = 128, WIN = 2048, NQ = 4096, F = 1792;
constexpr u32 SENT = 0xFFFFFFFFu;
constexpr u32 SPIN_CAP = 1u << 18;
constexpr float EPS = 1e-5f;

constexpr int EP_OFF = 0;
constexpr int Q1_OFF = 1024;
constexpr int Q2_OFF = Q1_OFF + 32 * 4096;
constexpr int AO_ST = 1152;
constexpr int AO_OFF = Q2_OFF + 32 * 1024;
constexpr int WO_OFF = AO_OFF + 2 * 32 * 8 * AO_ST;
constexpr int H_OFF = WO_OFF + 2 * 4 * 8 * 1024;
constexpr int X2_OFF = H_OFF + 2 * 4 * 4096;
constexpr int HT_OFF = X2_OFF + 2 * 4 * 8 * 1024;
constexpr int WS_BYTES = HT_OFF + 4 * 8 * 512;
constexpr int ST_OFF = 4 << 20;

#define PAR(i, v) (v)
#define P_AO0 PAR(0, 40)
#define P_WO0 PAR(1, 10)
#define P_H0 PAR(2, 7)
#define P_X PAR(3, 11)
#define P_Q PAR(4, 8)
#define P_AO1 PAR(5, 7)
#define P_WO1 PAR(6, 13)
#define P_H1 PAR(7, 7)
#define P_D PAR(8, 5)
#define P_R PAR(9, 2)
#define P_R0 PAR(10, 3)
#define P_F PAR(11, 7)

#define NK0 4
#ifndef NV0
#define NV0 0
#endif
#define NKF 0
#ifndef NVF
#define NVF 0
#endif
#define TQ 2
#define NO0 0
#define ND2 0
#define TCW 7
#define TCH 1
#define NW1 4
#define NW2 0
#define VBAR 1
#define MEMBAR() asm volatile("" ::: "memory")
#define SCHED() __builtin_amdgcn_sched_barrier(0)

__device__ __forceinline__ float asf(u32 x) { return __builtin_bit_cast(float, x); }
__device__ __forceinline__ u32 asu(float x) { return __builtin_bit_cast(u32, x); }
__device__ __forceinline__ u32 pk_bf16(float lo, float hi) {
  f32x2 f = {lo, hi};
  return __builtin_bit_cast(u32, __builtin_convertvector(f, bf16x2));
}
__device__ __forceinline__ float bf_lo(u32 x) { return asf(x << 16); }
__device__ __forceinline__ float bf_hi(u32 x) { return asf(x & 0xFFFF0000u); }
__device__ __forceinline__ u32 clean(u32 x) { return x == SENT ? 0x7FC07FC0u : x; }
__device__ __forceinline__ float dot2(u32 a, u32 b, float c) {
  return __builtin_amdgcn_fdot2_f32_bf16(__builtin_bit_cast(bf16x2, a), __builtin_bit_cast(bf16x2, b), c, false);
}
template <int CTRL> __device__ __forceinline__ u32 dppu(u32 x) {
  return (u32)__builtin_amdgcn_update_dpp(0, (int)x, CTRL, 0xf, 0xf, true);
}
template <int CTRL> __device__ __forceinline__ float dppf(float x) { return asf(dppu<CTRL>(asu(x))); }
constexpr int DPP_X1 = 0xB1, DPP_X2 = 0x4E, DPP_HM = 0x141, DPP_R8 = 0x128, DPP_SHL4 = 0x104;
__device__ __forceinline__ float sum8(float x) {
  x += dppf<DPP_X1>(x); x += dppf<DPP_X2>(x); x += dppf<DPP_HM>(x); return x;
}
__device__ __forceinline__ float vmax(float a, float b) { float r; asm("v_max_f32 %0, %1, %2" : "=v"(r) : "v"(a), "v"(b)); return r; }
__device__ __forceinline__ float max8(float x) {
  float r;
  asm("s_nop 1\n v_max_f32_dpp %0, %1, %1 quad_perm:[1,0,3,2] row_mask:0xf bank_mask:0xf\n"
      "s_nop 1\n v_max_f32_dpp %0, %0, %0 quad_perm:[2,3,0,1] row_mask:0xf bank_mask:0xf\n"
      "s_nop 1\n v_max_f32_dpp %0, %0, %0 row_half_mirror row_mask:0xf bank_mask:0xf" : "=&v"(r) : "v"(x));
  return r;
}
__device__ __forceinline__ float fold32(float a, float b) {
  auto r = __builtin_amdgcn_permlane32_swap(asu(a), asu(b), false, false);
  return asf(r[0]) + asf(r[1]);
}
__device__ __forceinline__ float fold16(float a, float b) {
  auto r = __builtin_amdgcn_permlane16_swap(asu(a), asu(b), false, false);
  return asf(r[0]) + asf(r[1]);
}
__device__ __forceinline__ float max32(float a) {
  auto r = __builtin_amdgcn_permlane32_swap(asu(a), asu(a), false, false);
  return vmax(asf(r[0]), asf(r[1]));
}
__device__ __forceinline__ float max16(float a) {
  auto r = __builtin_amdgcn_permlane16_swap(asu(a), asu(a), false, false);
  return vmax(asf(r[0]), asf(r[1]));
}
__device__ __forceinline__ float sum_hi(float x) {
  x += dppf<DPP_R8>(x); x = fold16(x, x); x = fold32(x, x); return x;
}
__device__ __forceinline__ float fold8(const float (&a)[8], int lane) {
  float b0 = fold32(a[0], a[4]), b1 = fold32(a[1], a[5]), b2 = fold32(a[2], a[6]), b3 = fold32(a[3], a[7]);
  float c0 = fold16(b0, b2), c1 = fold16(b1, b3);
  c0 += dppf<DPP_R8>(c0); c1 += dppf<DPP_R8>(c1);
  return (lane & 8) ? c1 : c0;
}
__device__ __forceinline__ u32 perm_lo(u32 r0, u32 r1) { return __builtin_amdgcn_perm(r1, r0, 0x05040100u); }
__device__ __forceinline__ u32 perm_hi(u32 r0, u32 r1) { return __builtin_amdgcn_perm(r1, r0, 0x07060302u); }
__device__ __forceinline__ void transpose8(const v4u (&r)[8], v4u (&t)[8]) {
#pragma unroll
  for (int c = 0; c < 4; ++c)
#pragma unroll
    for (int m = 0; m < 4; ++m) {
      t[2 * c][m] = perm_lo(r[2 * m][c], r[2 * m + 1][c]);
      t[2 * c + 1][m] = perm_hi(r[2 * m][c], r[2 * m + 1][c]);
    }
}
__device__ __forceinline__ bool ok4(v4u v) { return (v.x != SENT) & (v.y != SENT) & (v.z != SENT) & (v.w != SENT); }
__device__ __forceinline__ bool ok2(v2u v) { return (v.x != SENT) & (v.y != SENT); }

#define LD4(off) __builtin_amdgcn_raw_buffer_load_b128(rs, (off), 0, 16)
#define LD1(off) __builtin_amdgcn_raw_buffer_load_b32(rs, (off), 0, 16)
#define LD2(off) __builtin_amdgcn_raw_buffer_load_b64(rs, (off), 0, 16)
#define ST4(v, off) __builtin_amdgcn_raw_buffer_store_b128((v), rs, (off), 0, 16)
#define ST2(v, off) __builtin_amdgcn_raw_buffer_store_b64((v), rs, (off), 0, 16)

#define DW 0
#define STAMP(n) do { } while (0)

#define BAR() asm volatile("s_waitcnt lgkmcnt(0)\n s_barrier" ::: "memory")
#define NAP(n) do { _Pragma("nounroll") for (int i_ = (n); i_ > 0; --i_) __builtin_amdgcn_s_sleep(1); } while (0)
#define PIN8(a) asm volatile("" : "+v"(a[0]), "+v"(a[1]), "+v"(a[2]), "+v"(a[3]), "+v"(a[4]), "+v"(a[5]), "+v"(a[6]), "+v"(a[7]) :: "memory")
#define POLL(load, good, pause) do { MEMBAR(); load; \
  if (__builtin_expect(!__all(good), 0)) { _Pragma("nounroll") for (u32 spins = 0; spins < SPIN_CAP; ++spins) { \
    NAP(pause); MEMBAR(); if (!(good)) { load; } if (__all(good)) break; } } } while (0)
#define NTL4(p) __builtin_nontemporal_load((const v4u*)(p))
#define NTL2(p) __builtin_nontemporal_load((const v2u*)(p))
#define MFMA(a, b, c) __builtin_amdgcn_mfma_f32_16x16x32_bf16(__builtin_bit_cast(bf16x8, a), __builtin_bit_cast(bf16x8, b), c, 0, 0, 0)
constexpr u32 ONES = 0x3F803F80u;

struct __attribute__((aligned(16))) Lds {
  float ob[8][16 * 36];
  f32x2 ml[8][4];
  float rq[8][64];
  float rc[8][64];
  float ro[8][64];
  float rh[8][64];
  float c1[8][64];
  float rx[7][64];
  float sq[8][2];
  u32 q1[256];
};

__device__ __forceinline__ float wave_stats(u32 x0, u32 x1, u32 x2, u32 x3) {
  float s = dot2(x1, ONES, dot2(x0, ONES, 0.f)) + dot2(x3, ONES, dot2(x2, ONES, 0.f));
  float q = dot2(x1, x1, dot2(x0, x0, 0.f)) + dot2(x3, x3, dot2(x2, x2, 0.f));
  float t = fold32(s, q);
  t = fold16(t, t);
  return t + dppf<DPP_R8>(t);
}
__device__ __forceinline__ v4u pack8(float v) {
  u32 h0 = clean(pk_bf16(v, dppf<DPP_X1>(v)));
  u32 h1 = dppu<0xAA>(h0);
  v4u r = {h0, h1, dppu<DPP_SHL4>(h0), dppu<DPP_SHL4>(h1)};
  return r;
}

__global__ void __launch_bounds__(512, 2)
fused_decode(unsigned char* ws, const u16* __restrict__ x_in, const u16* __restrict__ wq,
             const u16* __restrict__ kc, const u16* __restrict__ vc, const u16* __restrict__ b1,
             const u16* __restrict__ b2, const u16* __restrict__ wo, const u16* __restrict__ w1,
             const u16* __restrict__ w2, u16* __restrict__ out) {
  __shared__ Lds L;
  STAMP(0);
  const int tid = threadIdx.x, lane = tid & 63;
  const int w = __builtin_amdgcn_readfirstlane(tid >> 6);
  const int hkv = blockIdx.x & 7, b = blockIdx.x >> 6, j = (blockIdx.x >> 3) & 7;
  const int jo = (j + 3) & 7, j2 = (j + 6) & 7;
  const int ci = hkv * 8 + j;
  const int grp = b * 8 + hkv;
  const int p = lane & 7, rg = lane >> 3, c16 = lane & 15, g = lane >> 4;
  const __amdgpu_buffer_rsrc_t rs = __builtin_amdgcn_make_buffer_rsrc(ws, 0, WS_BYTES, 0x00020000);

  {
    int off = -1;
    if (tid < 72) off = AO_OFF + (grp * 8 + j) * AO_ST + tid * 16;
    else if (tid < 144) off = AO_OFF + ((32 + grp) * 8 + j) * AO_ST + (tid - 72) * 16;
    else if (tid < 160) off = WO_OFF + ((((tid >> 3) & 1) * 4 + b) * 8 + hkv) * 1024 + jo * 128 + (tid & 7) * 16;
    else if (tid < 168) off = Q2_OFF + grp * 1024 + j * 128 + (tid & 7) * 16;
    else if (tid < 176) off = H_OFF + (((tid >> 2) & 1) * 4 + b) * 4096 + ci * 64 + (tid & 3) * 16;
    else if (tid < 192) off = X2_OFF + ((((tid >> 3) & 1) * 4 + b) * 8 + hkv) * 1024 + j2 * 128 + (tid & 7) * 16;
    if (off >= 0) { v4u s = {SENT, SENT, SENT, SENT}; ST4(s, off); }
  }
  MEMBAR();
  const size_t kvbase = ((size_t)grp * SK + (SK - WIN) + 256 * j + 32 * w) * DH;
  u32 cpf = 0;
#define TOUCH() if (w == TCW && lane < 6 && lane >= TQ) { \
    const int t = b * 8 + j + (lane & 1) * 32; \
    const u16* tp = lane < 2 ? wq + t * 32768 + hkv * 512 : lane == 2 ? kc + kvbase : lane == 3 ? vc + kvbase : t < 8 ? wo + (size_t)hkv * 512 * D + t * 32768 \
                  : t < 36 ? w1 + (t - 8) * 32768 + 64 * hkv : t < 41 ? w2 + (size_t)224 * hkv * D + (t - 36) * (224 * D / 4 - 64) \
                  : (const u16*)out; \
    cpf = __builtin_nontemporal_load((const u32*)tp); }
  TOUCH();
  const v2u ep = LD2(EP_OFF + b * 256);
  const v4u xin = *(const v4u*)(x_in + b * D + 64 * w + 8 * rg);
  v4u tq[8], kf[8], tv[8], to[8];
  v2u t1[8];
  v2u t2[8];
  v4u rq[8], rv[8], ro[8], r2[4];
  v2u r1[8];
  const u16* pk = kc + kvbase + (8 * (c16 >> 2) + (c16 & 3)) * DH + 8 * g;
#pragma unroll
  for (int i = 0; i < NKF; ++i) kf[i] = NTL4(pk + (i >> 2) * 4 * DH + (i & 3) * 32);
  const u16* pv = vc + kvbase + (8 * g) * DH + 8 * c16;
#pragma unroll
  for (int i = 0; i < NVF; ++i) rv[i] = NTL4(pv + i * DH);
  MEMBAR(); SCHED();
  {
    const u16* pq = wq + (size_t)(64 * w + 8 * rg) * NQ + hkv * 512 + 64 * j + 8 * p;
#pragma unroll
    for (int i = 0; i < 8; ++i) rq[i] = NTL4(pq + (size_t)i * NQ);
  }
  const int c1 = 224 * hkv + 32 * (j < 7 ? j : 6);
  const float b1v = bf_lo(b1[c1 + (lane & 31)]);
  const u32 b2r = b2[64 * j2 + lane];
  MEMBAR(); SCHED();
  __builtin_amdgcn_s_barrier();
  MEMBAR(); SCHED();
#pragma unroll
  for (int i = NKF; i < NK0; ++i) kf[i] = NTL4(pk + (i >> 2) * 4 * DH + (i & 3) * 32);
#pragma unroll
  for (int i = NVF; i < NV0; ++i) rv[i] = NTL4(pv + i * DH);
  const u16* po = wo + (size_t)(hkv * 512 + 128 * (w >> 1) + 32 * (2 * (w & 1) + (rg >> 2)) + 2 * (rg & 3)) * D + 64 * jo + 8 * p;
#pragma unroll
  for (int i = 0; i < NO0; ++i) ro[i] = NTL4(po + (8 * (i & 3) + (i >> 2)) * D);
  MEMBAR(); SCHED();
  transpose8(rq, tq);
  STAMP(1);

  {
    float a[8], c[8];
#pragma unroll
    for (int n = 0; n < 8; ++n) {
      a[n] = 0.f; c[n] = 0.f;
#pragma unroll
      for (int m = 0; m < 4; ++m) { a[n] = dot2(tq[n][m], xin[m], a[n]); c[n] = dot2(tq[n][m], ONES, c[n]); }
    }
    L.rq[w][8 * p + rg] = fold8(a, lane);
    L.rc[w][8 * p + rg] = fold8(c, lane);
    float st = wave_stats(xin.x, xin.y, xin.z, xin.w);
    if ((lane & 31) == 0) L.sq[w][lane >> 5] = st;
  }
  BAR();
  float csq = 0.f;
  const u32 tag_lo = (ep.x + 1u) & 0xFFFFu, tag_hi = ep.y + (tag_lo == 0u ? 1u : 0u);
  if (w == 0) {
    float s = 0.f;
#pragma unroll
    for (int u = 0; u < 8; ++u) { s += L.rq[u][lane]; csq += L.rc[u][lane]; }
    f32x2 t = *(const f32x2*)L.sq[p];
    float mean = sum8(t.x) * (1.0f / 512.0f);
    float var = vmax(fmaf(sum8(t.y), 1.0f / 512.0f, -mean * mean), 0.f);
    float qv = (s - mean * csq) * __builtin_amdgcn_rsqf(var + EPS);
    u32 qb = (pk_bf16(qv, qv) << 16) | tag_lo;
    u32 nb = dppu<DPP_X1>(qb);
    v4u gr = {qb, tag_hi, nb, tag_hi};
    if ((lane & 1) == 0) ST4(gr, Q1_OFF + grp * 4096 + (64 * j + lane) * 8);
  }
  STAMP(2);
  MEMBAR(); SCHED();
#define QOK(q) ((((q).x & 0xFFFFu) == tag_lo) & ((q).y == tag_hi) & (((q).z & 0xFFFFu) == tag_lo) & ((q).w == tag_hi))
  const int rh_idx = (lane & 8) ? 32 + (lane >> 5) : 4 * p + (lane >> 4);
  const bool rh_on = !(lane & 8) | ((lane & 31) == 8);
  const u16* p1 = w1 + (size_t)(64 * w + 8 * rg) * F + c1 + 4 * p;
  const u16* p2 = w2 + (size_t)(224 * hkv + 32 * (w < 7 ? w : 6) + 4 * rg) * D + 64 * j2 + 8 * p;
  {
#pragma unroll
    for (int i = NK0; i < 8; ++i) kf[i] = NTL4(pk + (i >> 2) * 4 * DH + (i & 3) * 32);
#pragma unroll
    for (int i = NV0; i < 8; ++i) rv[i] = NTL4(pv + i * DH);
    MEMBAR(); SCHED();
    if (VBAR) __builtin_amdgcn_s_barrier();
    const int qoff = Q1_OFF + grp * 4096 + (tid & 255) * 16;
    v4u qg = LD4(qoff);
    MEMBAR(); SCHED();
#pragma unroll
    for (int i = NO0; i < 8; ++i) ro[i] = NTL4(po + (8 * (i & 3) + (i >> 2)) * D);
#pragma unroll
    for (int i = 0; i < NW1; ++i) r1[i] = NTL2(p1 + i * F);
#pragma unroll
    for (int i = 0; i < NW2; ++i) r2[i] = NTL4(p2 + i * D);
    MEMBAR(); SCHED();
    STAMP(3);
    transpose8(rv, tv);
    PIN8(tv);
    if (w < 4) {
      if (__builtin_expect(!__all(QOK(qg)), 0)) {
        for (u32 spins = 0; spins < SPIN_CAP; ++spins) {
          NAP(P_R);
          MEMBAR();
          qg = LD4(qoff);
          if (__all(QOK(qg))) break;
        }
      }
      L.q1[tid] = (qg.x >> 16) | (qg.z & 0xFFFF0000u);
    }
  }
  asm volatile("" :: "v"(cpf));
  BAR();
  STAMP(4);
  const u32 t_own = (u32)__builtin_amdgcn_s_memrealtime() << 16;
  if (w == 7 && lane < 8) { v2u t = {tag_lo | t_own, tag_hi}; ST2(t, HT_OFF + (b * 8 + lane) * 512 + ci * 8); }

  v4u xb = xin;
  float cs1 = 0.f;

#pragma unroll
  for (int ly = 0; ly < 2; ++ly) {
    v4u qf[4];
    if (ly == 0) {
      qf[0] = *(const v4u*)&L.q1[(c16 & 3) * 64 + (c16 >> 2) * 16 + 4 * g];
    } else {
      const int off = Q2_OFF + grp * 1024 + (c16 & 3) * 256 + (c16 >> 2) * 64 + 16 * g;
      NAP(P_Q);
      POLL(qf[0] = LD4(off), ok4(qf[0]), P_R);
    }
#pragma unroll
    for (int c = 0; c < 4; ++c) {
      qf[1][c] = dppu<0x104>(qf[0][c]); qf[2][c] = dppu<0x108>(qf[0][c]); qf[3][c] = dppu<0x10C>(qf[0][c]);
    }
    STAMP(5 + 9 * ly);
    {
      f32x4 s0 = {0.f, 0.f, 0.f, 0.f}, s1 = {0.f, 0.f, 0.f, 0.f};
#pragma unroll
      for (int s = 0; s < 4; ++s) { s0 = MFMA(kf[s], qf[s], s0); s1 = MFMA(kf[4 + s], qf[s], s1); }
      const float sc = 0.08838834764831845f * 1.4426950408889634f;
      float m = fmaxf(fmaxf(fmaxf(fmaxf(s0[0], s0[1]), s0[2]), fmaxf(fmaxf(s0[3], s1[0]), s1[1])), fmaxf(s1[2], s1[3]));
      m = max32(max16(m)) * sc;
      float e[8];
#pragma unroll
      for (int r = 0; r < 4; ++r) {
        e[r] = __builtin_amdgcn_exp2f(fmaf(s0[r], sc, -m));
        e[4 + r] = __builtin_amdgcn_exp2f(fmaf(s1[r], sc, -m));
      }
      v4u pf = {pk_bf16(e[0], e[1]), pk_bf16(e[2], e[3]), pk_bf16(e[4], e[5]), pk_bf16(e[6], e[7])};
      const f32x4 z = {0.f, 0.f, 0.f, 0.f};
      const v4u ones = {ONES, ONES, ONES, ONES};
      f32x4 ls = MFMA(ones, pf, z);
      f32x4 o[8];
#pragma unroll
      for (int d = 0; d < 8; ++d) o[d] = MFMA(tv[d], pf, z);
      if (c16 < 4) {
        if (g == 0) { f32x2 t = {m, ls[0]}; L.ml[w][c16] = t; }
#pragma unroll
        for (int d = 0; d < 8; ++d) *(f32x4*)&L.ob[w][(c16 * 4 + g) * 36 + d * 4] = o[d];
      }
    }
    STAMP(23); BAR(); STAMP(24);
    {
      f32x2 t = L.ml[p][w >> 1];
      float ov[8];
#pragma unroll
      for (int u = 0; u < 8; ++u) ov[u] = L.ob[u][(tid >> 5) * 36 + (tid & 31)];
      float mx = max8(t.x);
      float e = __builtin_amdgcn_exp2f(t.x - mx);
      float lt = sum8(e * t.y);
      e *= __builtin_amdgcn_rcpf(lt);
      float o0 = 0.f, o1 = 0.f;
#pragma unroll
      for (int u = 0; u < 4; ++u) {
        o0 = fmaf(asf(__builtin_amdgcn_readlane(asu(e), u)), ov[u], o0);
        o1 = fmaf(asf(__builtin_amdgcn_readlane(asu(e), 4 + u)), ov[4 + u], o1);
      }
      v4u v = pack8(o0 + o1);
      const u32 lse = clean(asu(mx + __builtin_amdgcn_logf(lt)));
      if (lane == 1) v.x = lse;
      const int off = AO_OFF + ((ly * 32 + grp) * 8 + j) * AO_ST + (lane == 1 ? 1024 + 16 * w : tid * 2);
      if ((p == 0) | (lane == 1)) ST4(v, off);
    }
    STAMP(6 + 9 * ly);
    {
      u32 sv; v4u a0;
      const int off = AO_OFF + ((ly * 32 + grp) * 8 + p) * AO_ST;
      if (ly == 0) {
        MEMBAR(); SCHED();
#pragma unroll
        for (int i = NW1; i < 8; ++i) r1[i] = NTL2(p1 + i * F);
#pragma unroll
        for (int i = NW2; i < ND2; ++i) r2[i] = NTL4(p2 + i * D);
        v2u t = LD2(HT_OFF + (b * 8 + hkv) * 512 + lane * 8);
        MEMBAR(); SCHED();
        PIN8(ro);
    transpose8(ro, to);
    PIN8(to);
#pragma unroll
        for (int c = 0; c < 2; ++c)
#pragma unroll
          for (int m = 0; m < 4; ++m) {
            t1[4 * c + (m >> 1)][m & 1] = perm_lo(r1[2 * m][c], r1[2 * m + 1][c]);
            t1[4 * c + 2 + (m >> 1)][m & 1] = perm_hi(r1[2 * m][c], r1[2 * m + 1][c]);
          }
        float c[4];
#pragma unroll
        for (int n = 0; n < 4; ++n) {
          c[n] = 0.f;
#pragma unroll
          for (int m = 0; m < 4; ++m) c[n] = dot2(t1[2 * n + (m >> 1)][m & 1], ONES, c[n]);
        }
        float v = fold16(fold32(c[0], c[2]), fold32(c[1], c[3]));
        v += dppf<DPP_R8>(v);
        if ((lane & 8) == 0) L.c1[w][rh_idx] = v;
        if (w == 7) {
#define HOK() ((((t.x & 0xFFFFu) == tag_lo) & (t.y == tag_hi)) | (lane == ci))
          if (!__all(HOK())) {
            for (u32 spins = 0; spins < SPIN_CAP; ++spins) {
              NAP(P_R0);
              MEMBAR();
              if (!HOK()) t = LD2(HT_OFF + (b * 8 + hkv) * 512 + lane * 8);
              if (__all(HOK())) break;
            }
          }
          const u32 tt = lane == ci ? t_own : (t.x & 0xFFFF0000u);
          for (int n = 0; n < 100; ++n) {
            const u32 now = (u32)__builtin_amdgcn_s_memrealtime() << 16;
            if (__all(now - tt >= (u32)(P_AO0) << 18)) break;
            __builtin_amdgcn_s_sleep(1);
          }
        }
        BAR();
      } else NAP(P_AO1);
      POLL((sv = LD1(off + 1024 + 16 * w), a0 = LD4(off + (64 * w + 8 * rg) * 2)), ok4(a0) & (sv != SENT), P_R);
      STAMP(7 + 9 * ly);
      if (ly == 0) {
        MEMBAR(); SCHED();
#pragma unroll
        for (int i = ND2; i < 4; ++i) r2[i] = NTL4(p2 + i * D);
        MEMBAR(); SCHED();
      }
      float e = __builtin_amdgcn_exp2f(asf(sv) - max8(asf(sv)));
      e *= __builtin_amdgcn_rcpf(sum8(e));
      u32 ob[4];
#pragma unroll
      for (int m = 0; m < 4; ++m) ob[m] = pk_bf16(sum8(bf_lo(a0[m]) * e), sum8(bf_hi(a0[m]) * e));
      float a[8];
#pragma unroll
      for (int n = 0; n < 8; ++n) {
        a[n] = 0.f;
#pragma unroll
        for (int m = 0; m < 4; ++m) a[n] = dot2(to[n][m], ob[m], a[n]);
      }
      L.ro[w][8 * p + rg] = fold8(a, lane);
    }
    STAMP(25); BAR(); STAMP(26);
    if (w == 0) {
      float u0 = L.ro[0][lane], u1 = L.ro[1][lane], u2 = L.ro[2][lane], u3 = L.ro[3][lane];
      float u4 = L.ro[4][lane], u5 = L.ro[5][lane], u6 = L.ro[6][lane], u7 = L.ro[7][lane];
      v4u v = pack8(((u0 + u1) + (u2 + u3)) + ((u4 + u5) + (u6 + u7)));
      if (p == 0) ST4(v, WO_OFF + ((ly * 4 + b) * 8 + hkv) * 1024 + (64 * jo + lane) * 2);
    } else NAP(P_D);
    STAMP(8 + 9 * ly);
    {
      v4u a0;
      const int off = WO_OFF + ((ly * 4 + b) * 8 + p) * 1024 + (64 * w + 8 * rg) * 2;
      NAP(ly == 0 ? P_WO0 : P_WO1);
      POLL(a0 = LD4(off), ok4(a0), P_R);
      STAMP(9 + 9 * ly);
#pragma unroll
      for (int m = 0; m < 4; ++m) xb[m] = pk_bf16(bf_lo(xb[m]) + sum8(bf_lo(a0[m])), bf_hi(xb[m]) + sum8(bf_hi(a0[m])));
      float a[4];
#pragma unroll
      for (int n = 0; n < 4; ++n) {
        a[n] = 0.f;
#pragma unroll
        for (int m = 0; m < 4; ++m) a[n] = dot2(t1[2 * n + (m >> 1)][m & 1], xb[m], a[n]);
      }
      float v = fold16(fold32(a[0], a[2]), fold32(a[1], a[3]));
      v += dppf<DPP_R8>(v);
      float st = wave_stats(xb.x, xb.y, xb.z, xb.w);
      if (rh_on) L.rh[w][rh_idx] = (lane & 8) ? st : v;
    }
    STAMP(27); BAR(); STAMP(28);
    if (w == 0) {
      float u0 = L.rh[0][lane], u1 = L.rh[1][lane], u2 = L.rh[2][lane], u3 = L.rh[3][lane];
      float u4 = L.rh[4][lane], u5 = L.rh[5][lane], u6 = L.rh[6][lane], u7 = L.rh[7][lane];
      if (ly == 0) {
#pragma unroll
        for (int u = 0; u < 8; ++u) cs1 += L.c1[u][lane];
      }
      float s = ((u0 + u1) + (u2 + u3)) + ((u4 + u5) + (u6 + u7));
      float mean = asf(__builtin_amdgcn_readlane(asu(s), 32)) * (1.0f / 512.0f);
      float var = vmax(fmaf(asf(__builtin_amdgcn_readlane(asu(s), 33)), 1.0f / 512.0f, -mean * mean), 0.f);
      float r1s = __builtin_amdgcn_rsqf(var + EPS);
      float r2s = __builtin_amdgcn_rsqf(var * r1s * r1s + EPS);
      float z = fmaf((s - mean * cs1), r1s * r2s, b1v);
      float u2e = z * fmaf(z * z, 0.044715f, 1.0f) * (-2.0f * 0.7978845608028654f * 1.4426950408889634f);
      float hv = z * __builtin_amdgcn_rcpf(1.0f + __builtin_amdgcn_exp2f(u2e));
      v4u v = pack8(hv);
      if (lane < 32 && p == 0 && j < 7) ST4(v, H_OFF + (ly * 4 + b) * 4096 + ci * 64 + lane * 2);
    } else NAP(P_D + 1);
    STAMP(10 + 9 * ly);
    if (ly == 0) {
#pragma unroll
        for (int c = 0; c < 4; ++c)
#pragma unroll
          for (int m = 0; m < 2; ++m) {
            t2[2 * c][m] = perm_lo(r2[2 * m][c], r2[2 * m + 1][c]);
            t2[2 * c + 1][m] = perm_hi(r2[2 * m][c], r2[2 * m + 1][c]);
          }
    PIN8(t2);
    }
    if (w < 7) {
      const int off = H_OFF + (ly * 4 + b) * 4096 + hkv * 512 + w * 64 + rg * 8;
      NAP(ly == 0 ? P_H0 : P_H1);
      v2u hv;
      POLL(hv = LD2(off), ok2(hv), P_R);
      STAMP(11 + 9 * ly);
      float a[8];
#pragma unroll
      for (int n = 0; n < 8; ++n) a[n] = dot2(t2[n][1], hv.y, dot2(t2[n][0], hv.x, 0.f));
      L.rx[w][8 * p + rg] = fold8(a, lane);
    }
    STAMP(29); BAR();
    if (w == 0) {
      float u0 = L.rx[0][lane], u1 = L.rx[1][lane], u2 = L.rx[2][lane], u3 = L.rx[3][lane];
      float u4 = L.rx[4][lane], u5 = L.rx[5][lane], u6 = L.rx[6][lane];
      v4u v = pack8(((u0 + u1) + (u2 + u3)) + ((u4 + u5) + (u6 + (hkv == 0 ? bf_lo(b2r) : 0.f))));
      if (p == 0) ST4(v, X2_OFF + ((ly * 4 + b) * 8 + hkv) * 1024 + (64 * j2 + lane) * 2);
    } else NAP(P_D);
    if (ly == 0) STAMP(12);
    if (ly == 0 || (hkv == j && w == j)) {
      const int off = X2_OFF + ((ly * 4 + b) * 8 + p) * 1024 + (64 * w + 8 * rg) * 2;
      v4u a0;
      NAP(ly == 0 ? P_X : P_F);
      POLL(a0 = LD4(off), ok4(a0), P_R);
#pragma unroll
      for (int m = 0; m < 4; ++m) xb[m] = pk_bf16(bf_lo(xb[m]) + sum8(bf_lo(a0[m])), bf_hi(xb[m]) + sum8(bf_hi(a0[m])));
      if (ly == 1) {
        if (p == 0) *(v4u*)(out + b * D + 64 * w + 8 * rg) = xb;
        if (ci == 0 && lane == 0) { v2u e2 = {tag_lo, tag_hi}; ST2(e2, EP_OFF + b * 256); }
      }
    }
    if (ly == 1) STAMP(21);
    if (ly == 0) {
      STAMP(13);
      float a[8];
#pragma unroll
      for (int n = 0; n < 8; ++n) {
        a[n] = 0.f;
#pragma unroll
        for (int m = 0; m < 4; ++m) a[n] = dot2(tq[n][m], xb[m], a[n]);
      }
      L.rq[w][8 * p + rg] = fold8(a, lane);
      float st = wave_stats(xb.x, xb.y, xb.z, xb.w);
      if ((lane & 31) == 0) L.sq[w][lane >> 5] = st;
      STAMP(30); BAR(); STAMP(31);
      if (w == 0) {
        f32x2 t = *(const f32x2*)L.sq[p];
        float u0 = L.rq[0][lane], u1 = L.rq[1][lane], u2 = L.rq[2][lane], u3 = L.rq[3][lane];
        float u4 = L.rq[4][lane], u5 = L.rq[5][lane], u6 = L.rq[6][lane], u7 = L.rq[7][lane];
        float mean = sum8(t.x) * (1.0f / 512.0f);
        float var = vmax(fmaf(sum8(t.y), 1.0f / 512.0f, -mean * mean), 0.f);
        float s = ((u0 + u1) + (u2 + u3)) + ((u4 + u5) + (u6 + u7));
        v4u v = pack8((s - mean * csq) * __builtin_amdgcn_rsqf(var + EPS));
        if (p == 0) ST4(v, Q2_OFF + grp * 1024 + (64 * j + lane) * 2);
      } else NAP(P_D);
      STAMP(22);
    }
  }
}

extern "C" void kernel_launch(void* const* d_in, const int* in_sizes, int n_in, void* d_out, int out_size,
                              void* d_ws, size_t ws_size, hipStream_t stream) {
  fused_decode<<<256, 512, 0, stream>>>((unsigned char*)d_ws, (const u16*)d_in[0], (const u16*)d_in[3],
                                        (const u16*)d_in[1], (const u16*)d_in[2], (const u16*)d_in[7],
                                        (const u16*)d_in[9], (const u16*)d_in[4], (const u16*)d_in[6],
                                        (const u16*)d_in[8], (u16*)d_out);
}
```
